```python
import torch

def _softmax(x, axis=-1):
    return torch.softmax(x, dim=axis)
def _gelu(x):
    return torch.nn.functional.gelu(x, approximate="tanh")
def _silu(x):
    return torch.nn.functional.silu(x)
def _sigmoid(x):
    return torch.sigmoid(x)
def _layernorm(x, axis=-1, eps=1e-5):
    m = x.mean(dim=axis, keepdim=True)
    v = x.var(dim=axis, keepdim=True, unbiased=False)
    return (x - m) / torch.sqrt(v + eps)
def _scatter_add(tgt, idx, src, axis=0):
    return tgt.clone().index_add_(axis, idx, src)
def _segment_sum(src, seg, n):
    out = torch.zeros((n,) + src.shape[1:], dtype=src.dtype)
    return out.index_add_(0, seg, src)
def _pad(x, pw):
    flat = tuple(v for pair in reversed(pw) for v in pair)
    return torch.nn.functional.pad(x, flat)

kernel_name = 'recurrent_scan__softmax_full_mha__mixer_channel_mlp__prompt'

def setup_inputs():

    _w = (lambda a: (lambda t: t.to(torch.bfloat16) if t.is_floating_point() else t)(torch.as_tensor(a)))
    def _A_setup():
        B, S, D = 1, 2048, 640
        return {
            "x": _w((lambda *s: torch.randn(*s, dtype=torch.bfloat16) * 0.05)(B, S, D)),
            "wqkv": _w((lambda *s: torch.randn(*s, dtype=torch.bfloat16) * 0.05)(D, 3 * D)),
            "wo": _w((lambda *s: torch.randn(*s, dtype=torch.bfloat16) * 0.05)(D, D)),
        }
    _A_in = _A_setup()

    def _B_setup():
        B, S, D, F = 1, 2048, 640, 1536
        return {
            "x": _w((lambda *s: torch.randn(*s, dtype=torch.bfloat16) * 0.05)(B, S, D)),
            "w1": _w((lambda *s: torch.randn(*s, dtype=torch.bfloat16) * 1.6)(D, F) * (D ** -0.5)),
            "b1": _w((lambda *s: torch.randn(*s, dtype=torch.bfloat16) * 0.8)(F)),
            "w2": _w((lambda *s: torch.randn(*s, dtype=torch.bfloat16) * 0.05)(F, D) * (F ** -0.5)),
            "b2": _w((lambda *s: torch.randn(*s, dtype=torch.bfloat16) * 0.05)(D)),
        }
    _B_in = _B_setup()

    return {
        "A_x": _A_in["x"],
        "A_wqkv": _A_in["wqkv"],
        "A_wo": _A_in["wo"],
        "B_x": _B_in["x"],
        "B_w1": _B_in["w1"],
        "B_b1": _B_in["b1"],
        "B_w2": _B_in["w2"],
        "B_b2": _B_in["b2"],
    }

def reference(A_x, A_wqkv, A_wo, B_x, B_w1, B_b1, B_w2, B_b2):
    def _A_ref(x, wqkv, wo):
        B, S, D = x.shape
        H = 32; Dh = D // H
        qkv = (lambda x, *d: x.permute(*d))((x @ wqkv).reshape(B, S, 3, H, Dh), 2, 0, 3, 1, 4)
        q, k, v = qkv[0], qkv[1], qkv[2]
        scale = Dh ** -0.5
        scores = torch.einsum("bhqd,bhkd->bhqk", q, k) * scale
        p = _softmax(scores, axis=-1)
        o = torch.einsum("bhqk,bhkd->bhqd", p, v)
        return (lambda x, *d: x.permute(*d))(o, 0, 2, 1, 3).reshape(B, S, D) @ wo

    def _B_ref(x, w1, b1, w2, b2):
        y = _layernorm(x, axis=-1)
        h = _gelu(torch.einsum("bsd,df->bsf", y, w1) + b1)
        return torch.einsum("bsf,fd->bsd", h, w2) + b2

    x = A_x
    xn = _layernorm(x, axis=-1)
    s = _A_ref(xn, A_wqkv, A_wo)
    g = _silu(_B_ref(xn, B_w1, B_b1, B_w2, B_b2))
    return x + s * g
```

```cpp
#include <hip/hip_runtime.h>
#include <stdint.h>

typedef __attribute__((ext_vector_type(8))) short s16x8;
typedef __attribute__((ext_vector_type(4))) short s16x4;
typedef __attribute__((ext_vector_type(16))) float f32x16;
typedef __attribute__((ext_vector_type(4))) float f32x4;
typedef __attribute__((ext_vector_type(2))) float f32x2;
typedef __attribute__((ext_vector_type(4))) unsigned u32x4;
typedef __attribute__((ext_vector_type(2))) unsigned u32x2;
typedef __attribute__((ext_vector_type(2))) __bf16 bf16x2;
#define LAS __attribute__((address_space(3)))
#define DI __device__ __forceinline__
typedef LAS char* lds_ptr;
typedef __amdgpu_buffer_rsrc_t rsrc_t;

constexpr int S_ = 2048, D_ = 640, F_ = 1536, H_ = 32;
constexpr float EPS = 1e-5f;
constexpr float QSCALE = 0.22360679774997896f * 1.4426950408889634f;

constexpr unsigned WS_Q = 0;
constexpr unsigned WS_KV = WS_Q + 32u * 64u * 1280u;
constexpr unsigned WS_H = WS_KV + 32u * 64u * 2560u;
constexpr unsigned WS_O = WS_H + 2048u * 1536u * 2u;
constexpr unsigned WS_END = WS_O + 2048u * 640u * 2u;

constexpr int LDS_BYTES = 81920;

DI unsigned pack2(float a, float b) { return __builtin_bit_cast(unsigned, __builtin_convertvector((f32x2){a, b}, bf16x2)); }
DI float dot2(unsigned a, unsigned b, float c) { return __builtin_amdgcn_fdot2_f32_bf16(__builtin_bit_cast(bf16x2, a), __builtin_bit_cast(bf16x2, b), c, false); }
DI float bf2f(unsigned short v) { return __uint_as_float((unsigned)v << 16); }
DI rsrc_t mk_rsrc(const void* p, unsigned bytes) { return __builtin_amdgcn_make_buffer_rsrc((void*)p, (short)0, (int)bytes, 0x00020000); }
DI void dma16(rsrc_t rs, unsigned voff, unsigned soff, unsigned ldsaddr) {
  asm volatile("s_mov_b32 m0, %0\n\ts_nop 0\n\tbuffer_load_dwordx4 %1, %2, %3 offen lds" ::"s"(ldsaddr), "v"(voff), "s"(rs), "s"(soff) : "memory");
}
#define WAIT_BAR(N) asm volatile("s_waitcnt vmcnt(" #N ") lgkmcnt(0)\n\ts_barrier" ::: "memory")
#define SBAR() __builtin_amdgcn_sched_barrier(0)
#define MFMA32(a, b, c) __builtin_amdgcn_mfma_f32_32x32x16_bf16((a), (b), (c), 0, 0, 0)
#define MFMA16(a, b, c) __builtin_amdgcn_mfma_f32_16x16x32_bf16((a), (b), (c), 0, 0, 0)
DI s16x8 lds_b128(lds_ptr p) { return *(LAS const s16x8*)p; }
DI s16x4 lds_tr(lds_ptr p) { return __builtin_amdgcn_ds_read_tr16_b64_v4i16((LAS s16x4*)p); }
DI s16x8 cat8(s16x4 a, s16x4 b) { return __builtin_shufflevector(a, b, 0, 1, 2, 3, 4, 5, 6, 7); }
DI float swap_half_add(float v) {
  auto r = __builtin_amdgcn_permlane32_swap(__float_as_uint(v), __float_as_uint(v), false, false);
  return __uint_as_float(r[0]) + __uint_as_float(r[1]);
}
DI float sigmoid_mul(float u, float w  ) { return u * __builtin_amdgcn_rcpf(1.0f + __builtin_amdgcn_exp2f(w)); }

struct Params {
  const unsigned short* x;
  const unsigned short* wqkv;
  const unsigned short* wo;
  const unsigned short* w1;
  const unsigned short* b1;
  const unsigned short* w2;
  const unsigned short* b2;
  unsigned short* out;
  char* ws;
};

template <int MODE>
DI void gemm_xw_tile(const Params& p, int panel, int ct, lds_ptr lds) {
  constexpr int NT = MODE == 2 ? 3 : 4;
  constexpr unsigned LDW = MODE == 2 ? F_ * 2u : 3u * D_ * 2u;
  constexpr unsigned XS = 0, WSL = 49152;
  const int tid = threadIdx.x, lane = tid & 63, w = __builtin_amdgcn_readfirstlane(tid >> 6);
  const int r32 = lane & 31, hh = lane >> 5;
  const int n0 = ct * NT * 32;
  const unsigned lds0 = (unsigned)(uintptr_t)lds;
  unsigned xv[2];
#pragma unroll
  for (int i = 0; i < 2; ++i) {
    const int row = 8 * i + (lane >> 3);
    xv[i] = (unsigned)row * 1280u + (unsigned)(((lane & 7) ^ ((row >> 1) & 7)) * 16);
  }
  const unsigned xs0 = (unsigned)(panel * 128 + w * 32) * 1280u;
  unsigned wv;
  {
    const int row = lane >> 4, c = (lane & 15) ^ ((row & 3) << 2);
    wv = (unsigned)row * LDW + (unsigned)c * 16u + (unsigned)n0 * 2u;
    if (NT == 3 && c >= 12) wv = 0x80000000u;
  }
  auto issue_x = [&](int st, unsigned slot) {
    const unsigned so = xs0 + (unsigned)st * 128u;
    const rsrc_t rx = mk_rsrc(p.x, st < 10 ? S_ * D_ * 2 : 0);
#pragma unroll
    for (int i = 0; i < 4; ++i) dma16(rx, xv[i & 1], so + (unsigned)(i >> 1) * 20480u, lds0 + XS + slot + (unsigned)w * 4096u + (unsigned)i * 1024u);
  };
  auto issue_w = [&](int u, unsigned slot) {
    const unsigned so = (unsigned)(u * 32 + w * 8) * LDW;
    const rsrc_t rw = mk_rsrc(MODE == 2 ? p.w1 : p.wqkv, u < 20 ? D_ * LDW : 0);
#pragma unroll
    for (int i = 0; i < 2; ++i) dma16(rw, wv, so + (unsigned)i * 4u * LDW, lds0 + WSL + slot + (unsigned)w * 2048u + (unsigned)i * 1024u);
  };
  issue_x(0, 0); issue_w(0, 0); issue_w(1, 8192); issue_x(1, 16384); issue_w(2, 16384); issue_w(3, 24576); issue_x(2, 32768);
  unsigned xa[4], wa[NT];
  {
    const int row = w * 32 + r32, z = (row >> 1) & 7;
#pragma unroll
    for (int ks = 0; ks < 4; ++ks) xa[ks] = XS + (unsigned)row * 128u + (unsigned)(((2 * ks + hh) ^ z) * 16);
    const int gq = lane >> 4, q = (lane & 15) >> 2, pp = lane & 3;
#pragma unroll
    for (int i = 0; i < NT; ++i) {
      const int nt = (w + i) % NT;
      wa[i] = WSL + (unsigned)(8 * (gq >> 1) + q) * 256u + (unsigned)((nt ^ q) * 64 + (gq & 1) * 32 + pp * 8);
    }
  }
  f32x16 acc[NT];
#pragma unroll
  for (int i = 0; i < NT; ++i) acc[i] = f32x16{};
  float sx0 = 0.f, sx1 = 0.f, sq0 = 0.f, sq1 = 0.f, cs0 = 0.f, cs1 = 0.f;
  unsigned xslot = 0, xnext = 32768  , wbase = 0;
  auto compute = [&](int half, unsigned xo, unsigned wo) {
#pragma unroll
    for (int k2 = 0; k2 < 2; ++k2) {
      const s16x8 xf = lds_b128(lds + xa[half * 2 + k2] + xo);
      s16x8 wf[NT];
#pragma unroll
      for (int i = 0; i < NT; ++i) wf[i] = cat8(lds_tr(lds + wa[i] + wo + k2 * 4096), lds_tr(lds + wa[i] + wo + k2 * 4096 + 1024));
      const u32x4 xu = __builtin_bit_cast(u32x4, xf), wu = __builtin_bit_cast(u32x4, wf[0]);
#pragma unroll
      for (int i = 0; i < NT; ++i) acc[i] = MODE == 1 ? MFMA32(xf, wf[i], acc[i]) : MFMA32(wf[i], xf, acc[i]);
      sx0 = dot2(xu[0], 0x3F803F80u, sx0); sx1 = dot2(xu[1], 0x3F803F80u, sx1); sx0 = dot2(xu[2], 0x3F803F80u, sx0); sx1 = dot2(xu[3], 0x3F803F80u, sx1);
      sq0 = dot2(xu[0], xu[0], sq0); sq1 = dot2(xu[1], xu[1], sq1); sq0 = dot2(xu[2], xu[2], sq0); sq1 = dot2(xu[3], xu[3], sq1);
      cs0 = dot2(wu[0], 0x3F803F80u, cs0); cs1 = dot2(wu[1], 0x3F803F80u, cs1); cs0 = dot2(wu[2], 0x3F803F80u, cs0); cs1 = dot2(wu[3], 0x3F803F80u, cs1);
    }
  };
  for (int i = 0; i < 10; ++i) {
    if (i == 0) { WAIT_BAR(14); }
    else { WAIT_BAR(8); issue_w(2 * i + 3, (wbase ^ 16384) + 8192); issue_x(i + 2, xnext); }
    compute(0, xslot, wbase);
    WAIT_BAR(12);
    issue_w(2 * i + 4, wbase);
    compute(1, xslot, wbase + 8192);
    xnext = xslot; xslot = xslot == 32768 ? 0 : xslot + 16384; wbase ^= 16384;
  }
  const float mean = swap_half_add(sx0 + sx1) * (1.0f / D_);
  const float var = fmaxf(swap_half_add(sq0 + sq1) * (1.0f / D_) - mean * mean, 0.f);
  float rs = __builtin_amdgcn_rsqf(var + EPS);
  if (MODE == 2) rs *= __builtin_amdgcn_rsqf(var * rs * rs + EPS);
  const float csum = swap_half_add(cs0 + cs1);
  WAIT_BAR(0);
  LAS float* st_cs = (LAS float*)lds;
  LAS float* st_b = (LAS float*)(lds + 512);
  LAS float* st_rs = (LAS float*)(lds + 1024);
  LAS float* st_mr = (LAS float*)(lds + 1536);
  if (hh == 0 && w < NT) {
    st_cs[w * 32 + r32] = csum;
    if (MODE == 2) st_b[w * 32 + r32] = bf2f(p.b1[n0 + w * 32 + r32]);
  }
  if (MODE == 1 && hh == 0) { st_rs[w * 32 + r32] = rs; st_mr[w * 32 + r32] = mean * rs; }
  __syncthreads();
  const rsrc_t rws = mk_rsrc(p.ws, WS_END);
  if constexpr (MODE == 0) {
    const int sect = ct / 5;
    const float sc = sect == 0 ? rs * QSCALE : rs;
    const float ms = -mean;
    const unsigned tile = (unsigned)(panel * 4 + w);
    const unsigned tstride = sect == 0 ? 1280u : 2560u, base = sect == 0 ? WS_Q : WS_KV;
#pragma unroll
    for (int i = 0; i < NT; ++i) {
      const int nt = (w + i) % NT;
#pragma unroll
      for (int g = 0; g < 4; ++g) {
        const f32x4 c4 = *(LAS const f32x4*)(st_cs + nt * 32 + 8 * g + 4 * hh);
        float v[4];
#pragma unroll
        for (int e = 0; e < 4; ++e) v[e] = sc * __builtin_fmaf(ms, c4[e], acc[i][4 * g + e]);
        const unsigned quad = (unsigned)((ct % 5) * 32 + nt * 8 + 2 * g + hh), head = quad / 5u, dq = quad - head * 5u;
        const unsigned off = base + (head * 64u + tile) * tstride + (dq < 4 ? (dq >> 1) * 512u + (unsigned)r32 * 16u + (dq & 1) * 8u : 1024u + (unsigned)r32 * 8u);
        __builtin_amdgcn_raw_buffer_store_b64((u32x2){pack2(v[0], v[1]), pack2(v[2], v[3])}, rws, off, 0, 0);
      }
    }
  } else if constexpr (MODE == 1) {
    const unsigned tile = (unsigned)(panel * 4 + w);
#pragma unroll
    for (int i = 0; i < NT; ++i) {
      const int nt = (w + i) % NT;
      const float cs = -st_cs[nt * 32 + r32];
      const unsigned f = (unsigned)((ct % 5) * 128 + nt * 32 + r32), head = f / 20u, d = f - head * 20u;
      float v[16];
#pragma unroll
      for (int g = 0; g < 4; ++g) {
        const f32x4 r4 = *(LAS const f32x4*)(st_rs + w * 32 + 8 * g + 4 * hh), m4 = *(LAS const f32x4*)(st_mr + w * 32 + 8 * g + 4 * hh);
#pragma unroll
        for (int e = 0; e < 4; ++e) v[4 * g + e] = __builtin_fmaf(acc[i][4 * g + e], r4[e], m4[e] * cs);
      }
#pragma unroll
      for (int s = 0; s < 2; ++s) {
        const unsigned off = WS_KV + (head * 64u + tile) * 2560u + 1280u + (unsigned)((hh * 2 + s) * 20) * 16u + d * 16u;
        __builtin_amdgcn_raw_buffer_store_b128((u32x4){pack2(v[8 * s], v[8 * s + 1]), pack2(v[8 * s + 2], v[8 * s + 3]), pack2(v[8 * s + 4], v[8 * s + 5]), pack2(v[8 * s + 6], v[8 * s + 7])}, rws, off, 0, 0);
      }
    }
  } else {
    const float ms = -mean;
    const unsigned tok = (unsigned)(panel * 128 + w * 32 + r32);
    constexpr float G0 = -2.0f * 0.7978845608028654f * 1.4426950408889634f, G1 = G0 * 0.044715f;
#pragma unroll
    for (int i = 0; i < NT; ++i) {
      const int nt = (w + i) % NT;
      float v[16];
#pragma unroll
      for (int g = 0; g < 4; ++g) {
        const f32x4 c4 = *(LAS const f32x4*)(st_cs + nt * 32 + 8 * g + 4 * hh), b4 = *(LAS const f32x4*)(st_b + nt * 32 + 8 * g + 4 * hh);
#pragma unroll
        for (int e = 0; e < 4; ++e) {
          const float u = __builtin_fmaf(rs, __builtin_fmaf(ms, c4[e], acc[i][4 * g + e]), b4[e]);
          v[4 * g + e] = sigmoid_mul(u, u * __builtin_fmaf(u * u, G1, G0));
        }
      }
#pragma unroll
      for (int s = 0; s < 2; ++s) {
        const unsigned chunk = (unsigned)((n0 + nt * 32) / 8 + 2 * s + hh);
        const unsigned off = WS_H + ((tok >> 6) * 192u + chunk) * 1024u + (tok & 63u) * 16u;
        __builtin_amdgcn_raw_buffer_store_b128((u32x4){pack2(v[8 * s], v[8 * s + 1]), pack2(v[8 * s + 2], v[8 * s + 3]), pack2(v[8 * s + 4], v[8 * s + 5]), pack2(v[8 * s + 6], v[8 * s + 7])}, rws, off, 0, 0);
      }
    }
  }
}

__global__ __launch_bounds__(256, 2) void k_gemm_xw(Params p) {
  extern __shared__ __attribute__((aligned(16))) char smem[];
  lds_ptr lds = (lds_ptr)smem;
  const int b = blockIdx.x & 255, xcd = b & 7, j = b >> 3;
  const int panel = (xcd & 1) * 8 + (j & 7), c = (xcd >> 1) * 4 + (j >> 3);
  if (blockIdx.x < 256) {
    if (c >= 15) return;
    if (c < 10) gemm_xw_tile<0>(p, panel, c, lds);
    else gemm_xw_tile<1>(p, panel, c, lds);
  } else {
    gemm_xw_tile<2>(p, panel, c, lds);
  }
}

constexpr unsigned PAIR = 5456, SLOT = 4 * PAIR;

DI void attn_store_o(const Params& p, rsrc_t rws, const f32x16& o, float inv, unsigned tok, int head, int hh) {
  const unsigned base = WS_O + (tok >> 6) * 80u * 1024u + (tok & 63u) * 16u;
#pragma unroll
  for (int g = 0; g < 3; ++g) {
    const unsigned quad = (unsigned)(head * 5 + 2 * g + hh);
    const unsigned off = (g == 2 && hh) ? 0x80000000u : base + (quad >> 1) * 1024u + (quad & 1) * 8u;
    __builtin_amdgcn_raw_buffer_store_b64((u32x2){pack2(o[4 * g] * inv, o[4 * g + 1] * inv), pack2(o[4 * g + 2] * inv, o[4 * g + 3] * inv)}, rws, off, 0, 0);
  }
}

DI void attn_block(const Params& p, int head, int qt, lds_ptr lds) {
  const int tid = threadIdx.x, lane = tid & 63, w = __builtin_amdgcn_readfirstlane(tid >> 6);
  const int r32 = lane & 31, hh = lane >> 5;
  const unsigned lds0 = (unsigned)(uintptr_t)lds;
  const rsrc_t rws = mk_rsrc(p.ws, WS_END);
  const unsigned kv0 = WS_KV + (unsigned)head * 64u * 2560u + (unsigned)w * 5120u;
  auto issue_kv = [&](int c, unsigned slot) {
    const unsigned so = kv0 + (unsigned)c * 20480u;
#pragma unroll
    for (int i = 0; i < 5; ++i) dma16(rws, (unsigned)lane * 16u, so + (unsigned)i * 1024u, lds0 + slot + (unsigned)w * PAIR + (unsigned)i * 1024u);
  };
  {
    const unsigned qb = WS_Q + ((unsigned)head * 64u + (unsigned)(qt * 8 + w * 2)) * 1280u;
#pragma unroll
    for (int i = 0; i < 3; ++i) dma16(rws, (unsigned)lane * 16u, qb + (unsigned)i * 1024u, lds0 + 3 * SLOT + (unsigned)w * 3072u + (unsigned)i * 1024u);
  }
  issue_kv(0, 0); issue_kv(1, SLOT); issue_kv(2, 2 * SLOT);
  if (tid < 24) *(LAS u32x4*)(lds + (tid >> 1) * PAIR + 5120 + (tid & 1) * 320) = (u32x4){0x3F803F80u, 0x3F803F80u, 0x3F803F80u, 0x3F803F80u};
  s16x8 qf0[2], qf1[2];
  const unsigned ka0 = (unsigned)hh * 512u + (unsigned)r32 * 16u, ka1 = 1024u + (unsigned)r32 * 8u;
  const unsigned vaE = r32 < 20 ? 1280u + (unsigned)hh * 640u + (unsigned)r32 * 16u : 5120u;
  const unsigned vaO = r32 < 20 ? vaE : 2560u;
  f32x16 oa = f32x16{}, ob = f32x16{};
  f32x16 sA0, sA1, sB0, sB1;
  s16x8 k0[2], k1[2], v0[2], v1[2];
  u32x4 pa0, pa1, pb0, pb1;
#define TOFF(i) (((i) >> 1) * PAIR + ((i) & 1) * 2560)
#define RD_K(set, slot, i) do { k0[set] = lds_b128(lds + ka0 + (slot) + TOFF(i)); const u32x2 t_ = *(LAS const u32x2*)(lds + ka1 + (slot) + TOFF(i)); \
    k1[set] = __builtin_bit_cast(s16x8, (u32x4){t_[0], t_[1], 0u, 0u}); } while (0)
#define RD_V(set, slot, i) do { v0[set] = lds_b128(lds + (((i) & 1) ? vaO : vaE) + (slot) + TOFF(i)); v1[set] = lds_b128(lds + (((i) & 1) ? vaO : vaE) + (slot) + TOFF(i) + 320); } while (0)
#define EXP4(S, r, P, j) do { S[r] = __builtin_amdgcn_exp2f(S[r]); S[r + 1] = __builtin_amdgcn_exp2f(S[r + 1]); S[r + 2] = __builtin_amdgcn_exp2f(S[r + 2]); S[r + 3] = __builtin_amdgcn_exp2f(S[r + 3]); \
    P[j] = pack2(S[r], S[r + 1]); P[j + 1] = pack2(S[r + 2], S[r + 3]); asm volatile("" : "+v"(P)); SBAR(); } while (0)
#define PF(x) __builtin_bit_cast(s16x8, x)
  const f32x16 zero = f32x16{};
  WAIT_BAR(10);
#pragma unroll
  for (int a = 0; a < 2; ++a) {
    qf0[a] = lds_b128(lds + 3 * SLOT + w * 3072 + a * 1280 + ka0);
    const u32x2 t = *(LAS const u32x2*)(lds + 3 * SLOT + w * 3072 + a * 1280 + ka1);
    qf1[a] = __builtin_bit_cast(s16x8, (u32x4){hh ? 0u : t[0], hh ? 0u : t[1], 0u, 0u});
  }
  RD_K(0, 0, 0); RD_K(1, 0, 1); RD_V(0, 0, 0);
  sA0 = MFMA32(k0[0], qf0[0], zero); sA0 = MFMA32(k1[0], qf1[0], sA0);
  sA1 = MFMA32(k0[0], qf0[1], zero); sA1 = MFMA32(k1[0], qf1[1], sA1);
  pb1 = (u32x4){0u, 0u, 0u, 0u}; v1[1] = v0[0];
#define STEP(C0, C1, N0, N1, kc, vc, KS, KI, VS, VI) do { \
    ob = MFMA32(v1[vc ^ 1], PF(pb1), ob); SBAR(); \
    RD_K(kc ^ 1, KS, KI); RD_V(vc ^ 1, VS, VI); SBAR(); \
    EXP4(C0, 0, pa0, 0); \
    N0 = MFMA32(k0[kc], qf0[0], zero); SBAR(); EXP4(C0, 4, pa0, 2); \
    N0 = MFMA32(k1[kc], qf1[0], N0); SBAR(); EXP4(C0, 8, pa1, 0); \
    oa = MFMA32(v0[vc], PF(pa0), oa); SBAR(); EXP4(C0, 12, pa1, 2); \
    N1 = MFMA32(k0[kc], qf0[1], zero); SBAR(); EXP4(C1, 0, pb0, 0); \
    oa = MFMA32(v1[vc], PF(pa1), oa); SBAR(); EXP4(C1, 4, pb0, 2); \
    N1 = MFMA32(k1[kc], qf1[1], N1); SBAR(); EXP4(C1, 8, pb1, 0); \
    ob = MFMA32(v0[vc], PF(pb0), ob); SBAR(); EXP4(C1, 12, pb1, 2); \
  } while (0)
  unsigned cur = 0, nxt = SLOT;
  for (int c = 0; c < 8; ++c) {
    STEP(sA0, sA1, sB0, sB1, 1, 0, cur, 2, cur, 1);
    STEP(sB0, sB1, sA0, sA1, 0, 1, cur, 3, cur, 2);
    STEP(sA0, sA1, sB0, sB1, 1, 0, cur, 4, cur, 3);
    STEP(sB0, sB1, sA0, sA1, 0, 1, cur, 5, cur, 4);
    STEP(sA0, sA1, sB0, sB1, 1, 0, cur, 6, cur, 5);
    STEP(sB0, sB1, sA0, sA1, 0, 1, cur, 7, cur, 6);
    const unsigned prv = nxt == 2 * SLOT ? 0 : nxt + SLOT;
    if (c == 0) { WAIT_BAR(5); }
    else { WAIT_BAR(0); if (c < 6) issue_kv(c + 2, prv); }
    STEP(sA0, sA1, sB0, sB1, 1, 0, nxt, 0, cur, 7);
    STEP(sB0, sB1, sA0, sA1, 0, 1, nxt, 1, nxt, 0);
    cur = nxt; nxt = prv;
  }
  ob = MFMA32(v1[1], PF(pb1), ob);
  const float la = hh ? oa[8] : oa[12], lb = hh ? ob[8] : ob[12];
  const unsigned tok = (unsigned)(qt * 256 + w * 64 + r32);
  attn_store_o(p, rws, oa, __builtin_amdgcn_rcpf(la), tok, head, hh);
  attn_store_o(p, rws, ob, __builtin_amdgcn_rcpf(lb), tok + 32, head, hh);
}

__global__ __launch_bounds__(256, 2) void k_attn(Params p) {
  extern __shared__ __attribute__((aligned(16))) char smem[];
  const int b = blockIdx.x, xcd = b & 7, j = b >> 3;
  attn_block(p, xcd * 4 + (j >> 3), j & 7, (lds_ptr)smem);
}

constexpr unsigned ST2 = 20480;
template <bool PERM, int NST>
DI void gemm_64x80(const void* pa, unsigned a0, const void* pw, unsigned n0, lds_ptr lds, f32x4 (&acc)[5]) {
  const int tid = threadIdx.x, lane = tid & 63, w = __builtin_amdgcn_readfirstlane(tid >> 6);
  const unsigned lds0 = (unsigned)(uintptr_t)lds;
  unsigned wv[3];
#pragma unroll
  for (int i = 0; i < 3; ++i) {
    const int id = i * 64 + lane, row = id / 10, c = id - row * 10;
    wv[i] = id < 160 ? (unsigned)row * 1280u + (unsigned)c * 16u + n0 * 2u : 0x80000000u;
  }
  auto issue = [&](int st, unsigned slot) {
    const bool ok = st < NST;
    const unsigned sa = a0 + (unsigned)st * 8192u + (unsigned)w * 2048u;
    const unsigned sw = (unsigned)(st * 64 + w * 16) * 1280u;
    const rsrc_t ra = mk_rsrc(pa, ok ? WS_END : 0), rw = mk_rsrc(pw, ok ? NST * 64 * 1280 : 0);
    dma16(ra, (unsigned)lane * 16u, sa, lds0 + slot + (unsigned)w * 2048u);
    dma16(ra, (unsigned)lane * 16u, sa + 1024u, lds0 + slot + (unsigned)w * 2048u + 1024u);
#pragma unroll
    for (int i = 0; i < 3; ++i) dma16(rw, wv[i], sw, lds0 + slot + 8192u + (unsigned)w * 3072u + (unsigned)i * 1024u);
  };
  issue(0, 0); issue(1, ST2); issue(2, 2 * ST2);
  const int g = lane >> 4, q = (lane & 15) >> 2, pp = lane & 3;
  const unsigned aa = (unsigned)g * 1024u + (unsigned)(w * 16 + (lane & 15)) * 16u;
  const int wrow = PERM ? 4 * (g & 1) + q : 8 * (g & 1) + q;
  const unsigned wa = 8192u + (unsigned)(g >> 1) * 3072u + (unsigned)wrow * 160u + (unsigned)pp * 8u;
  constexpr unsigned TS = PERM ? 8 * 160 : 4 * 160;
  unsigned slot = 0, fill = 3 * ST2;
  for (int st = 0; st < NST; ++st) {
    WAIT_BAR(10);
    issue(st + 3, fill);
#pragma unroll
    for (int s = 0; s < 2; ++s) {
      const s16x8 af = lds_b128(lds + aa + slot + s * 4096);
#pragma unroll
      for (int j = 0; j < 5; ++j) {
        const s16x8 wf = cat8(lds_tr(lds + wa + slot + s * 6144 + j * 32), lds_tr(lds + wa + slot + s * 6144 + j * 32 + TS));
        acc[j] = MFMA16(wf, af, acc[j]);
      }
    }
    fill = slot; slot = slot == 3 * ST2 ? 0 : slot + ST2;
  }
  WAIT_BAR(0);
}

DI void tail_block(const Params& p, int rt, int cn, lds_ptr lds) {
  const int tid = threadIdx.x, lane = tid & 63, w = __builtin_amdgcn_readfirstlane(tid >> 6);
  const unsigned n0 = (unsigned)cn * 80u;
  f32x4 am[5], as[5];
#pragma unroll
  for (int j = 0; j < 5; ++j) { am[j] = f32x4{}; as[j] = f32x4{}; }
  gemm_64x80<true, 24>(p.ws, WS_H + (unsigned)rt * 192u * 1024u, p.w2, n0, lds, am);
  gemm_64x80<false, 10>(p.ws, WS_O + (unsigned)rt * 80u * 1024u, p.wo, n0, lds, as);
  const unsigned tok = (unsigned)(rt * 64 + w * 16 + (lane & 15)), g = (unsigned)lane >> 4;
#pragma unroll
  for (int j = 0; j < 5; ++j) {
    const unsigned n = n0 + j * 16 + g * 4;
    const u32x2 xb = *(const u32x2*)(p.x + tok * D_ + n), bb = *(const u32x2*)(p.b2 + n);
    float r[4];
#pragma unroll
    for (int e = 0; e < 4; ++e) {
      const float xe = __uint_as_float(e & 1 ? xb[e >> 1] & 0xFFFF0000u : xb[e >> 1] << 16), be = __uint_as_float(e & 1 ? bb[e >> 1] & 0xFFFF0000u : bb[e >> 1] << 16);
      const float m = am[j][e] + be;
      r[e] = __builtin_fmaf(as[j][e], sigmoid_mul(m, m * -1.4426950408889634f), xe);
    }
    *(u32x2*)(p.out + tok * D_ + n) = (u32x2){pack2(r[0], r[1]), pack2(r[2], r[3])};
  }
}

__global__ __launch_bounds__(256, 2) void k_tail(Params p) {
  extern __shared__ __attribute__((aligned(16))) char smem[];
  const int b = blockIdx.x, xcd = b & 7, j = b >> 3;
  tail_block(p, (xcd >> 1) * 8 + (j & 7), (xcd & 1) * 4 + (j >> 3), (lds_ptr)smem);
}

extern "C" void kernel_launch(void* const* d_in, const int* in_sizes, int n_in, void* d_out, int out_size, void* d_ws, size_t ws_size, hipStream_t stream) {
  Params p;
  p.x = (const unsigned short*)d_in[0];
  p.wqkv = (const unsigned short*)d_in[1];
  p.wo = (const unsigned short*)d_in[2];
  p.w1 = (const unsigned short*)d_in[4];
  p.b1 = (const unsigned short*)d_in[5];
  p.w2 = (const unsigned short*)d_in[6];
  p.b2 = (const unsigned short*)d_in[7];
  p.out = (unsigned short*)d_out;
  p.ws = (char*)d_ws;
  (void)hipFuncSetAttribute((const void*)k_gemm_xw, hipFuncAttributeMaxDynamicSharedMemorySize, LDS_BYTES);
  (void)hipFuncSetAttribute((const void*)k_attn, hipFuncAttributeMaxDynamicSharedMemorySize, LDS_BYTES);
  (void)hipFuncSetAttribute((const void*)k_tail, hipFuncAttributeMaxDynamicSharedMemorySize, LDS_BYTES);
  k_gemm_xw<<<512, 256, LDS_BYTES, stream>>>(p);
  k_attn<<<256, 256, LDS_BYTES, stream>>>(p);
  k_tail<<<256, 256, LDS_BYTES, stream>>>(p);
}
```

```cpp
#include <hip/hip_runtime.h>
#include <stdint.h>
#include <type_traits>
#define MLP_DELAY 1
#define NO_WARM 1

typedef __attribute__((ext_vector_type(8))) short s16x8;
typedef __attribute__((ext_vector_type(4))) short s16x4;
typedef __attribute__((ext_vector_type(16))) float f32x16;
typedef __attribute__((ext_vector_type(4))) float f32x4;
typedef __attribute__((ext_vector_type(2))) float f32x2;
typedef __attribute__((ext_vector_type(4))) unsigned u32x4;
typedef __attribute__((ext_vector_type(2))) unsigned u32x2;
typedef __attribute__((ext_vector_type(2))) __bf16 bf16x2;
#define LAS __attribute__((address_space(3)))
#define DI __device__ __forceinline__
typedef LAS char* lds_ptr;
typedef __amdgpu_buffer_rsrc_t rsrc_t;

constexpr int S_ = 2048, D_ = 640, F_ = 1536;
constexpr float EPS = 1e-5f;
constexpr float QSCALE = 0.22360679774997896f * 1.4426950408889634f;
constexpr int SC1 = 16;
constexpr unsigned OOB = 0x80000000u;

constexpr unsigned WS_Q = 0;
constexpr unsigned WS_KV = WS_Q + 32u * 64u * 1280u;
constexpr unsigned WS_H = WS_KV + 32u * 64u * 2560u;
constexpr unsigned WS_O = WS_H + 2048u * 1536u * 2u;
constexpr unsigned SY_GEN = WS_O + 2048u * 640u * 2u;
constexpr unsigned SY_START = SY_GEN + 256;
constexpr unsigned SY_GO = SY_START + 512 * 8;
constexpr unsigned SY_FQ = SY_GO + 256 * 8;
constexpr unsigned SY_FH = SY_FQ + 12 * 64 * 8;
constexpr unsigned SY_FO = SY_FH + 64 * 16 * 8;
constexpr unsigned SY_TOK = SY_FO + 32 * 32 * 8;
constexpr unsigned SY_COL = SY_TOK + 2048 * 16;
constexpr unsigned WS_END = SY_COL + 3456 * 16;

constexpr int LDS_BYTES = 81920;
constexpr unsigned SPIN_CAP = 1u << 22;

DI unsigned pack2(float a, float b) { return __builtin_bit_cast(unsigned, __builtin_convertvector((f32x2){a, b}, bf16x2)); }
DI float bf_lo(unsigned v) { return __uint_as_float(v << 16); }
DI float bf_hi(unsigned v) { return __uint_as_float(v & 0xFFFF0000u); }
DI rsrc_t mk_rsrc(const void* p, unsigned bytes) { return __builtin_amdgcn_make_buffer_rsrc((void*)p, (short)0, (int)bytes, 0x00020000); }
DI void dma16(rsrc_t rs, unsigned voff, unsigned soff, unsigned ldsaddr) {
  asm volatile("s_mov_b32 m0, %0\n\ts_nop 0\n\tbuffer_load_dwordx4 %1, %2, %3 offen lds" ::"s"(ldsaddr), "v"(voff), "s"(rs), "s"(soff) : "memory");
}
DI void dma16_sc1(rsrc_t rs, unsigned voff, unsigned soff, unsigned ldsaddr) {
  asm volatile("s_mov_b32 m0, %0\n\ts_nop 0\n\tbuffer_load_dwordx4 %1, %2, %3 offen sc1 lds" ::"s"(ldsaddr), "v"(voff), "s"(rs), "s"(soff) : "memory");
}
template <int N> DI void wait_bar() { asm volatile("s_waitcnt vmcnt(%0) lgkmcnt(0)\n\ts_barrier" ::"i"(N) : "memory"); }
DI void drain() { asm volatile("s_waitcnt vmcnt(0)" ::: "memory"); }
#define SBAR() __builtin_amdgcn_sched_barrier(0)
#define MFMA32(a, b, c) __builtin_amdgcn_mfma_f32_32x32x16_bf16((a), (b), (c), 0, 0, 0)
#define MFMA16(a, b, c) __builtin_amdgcn_mfma_f32_16x16x32_bf16((a), (b), (c), 0, 0, 0)
DI s16x8 lds_b128(lds_ptr p) { return *(LAS const s16x8*)p; }
DI s16x4 lds_tr(lds_ptr p) { return __builtin_amdgcn_ds_read_tr16_b64_v4i16((LAS s16x4*)p); }
DI s16x8 cat8(s16x4 a, s16x4 b) { return __builtin_shufflevector(a, b, 0, 1, 2, 3, 4, 5, 6, 7); }
DI float sigmoid_mul(float u, float w  ) { return u * __builtin_amdgcn_rcpf(1.0f + __builtin_amdgcn_exp2f(w)); }

struct Params {
  const unsigned short* x;
  const unsigned short* wqkv;
  const unsigned short* wo;
  const unsigned short* w1;
  const unsigned short* b1;
  const unsigned short* w2;
  const unsigned short* b2;
  unsigned short* out;
  char* ws;
};
struct Tag { unsigned a, b; };

DI Tag load_tag(const Params& p) {
  unsigned long long g;
  asm volatile("s_load_dwordx2 %0, %1, 0x0\n\ts_waitcnt lgkmcnt(0)" : "=s"(g) : "s"(p.ws + SY_GEN) : "memory");
  g += 0x9E3779B97F4A7C15ull; g = (g ^ (g >> 30)) * 0xBF58476D1CE4E5B9ull; g = (g ^ (g >> 27)) * 0x94D049BB133111EBull; g ^= g >> 31;
  return Tag{(unsigned)g, (unsigned)(g >> 32)};
}
DI void flag_store(rsrc_t rws, unsigned off, Tag t) {
  if ((threadIdx.x & 63) == 0) __builtin_amdgcn_raw_buffer_store_b64((u32x2){t.a, t.b}, rws, off, 0, SC1);
}
#define publish(rws, off, t, K, I) do {   drain(); flag_store(rws, off, t); } while (0)
template <int N> DI void poll_flags(rsrc_t rws, const unsigned (&off)[N], Tag t) {
  for (unsigned spin = 0; spin < SPIN_CAP; ++spin) {
    bool ok = true;
#pragma unroll
    for (int i = 0; i < N; ++i) {
      const u32x2 v = __builtin_amdgcn_raw_buffer_load_b64(rws, off[i], 0, SC1);
      ok = ok && (off[i] == OOB || (v[0] == t.a && v[1] == t.b));
    }
    if (__all(ok)) break;
    __builtin_amdgcn_s_sleep(2);
  }
  asm volatile("" ::: "memory");
}

template <int MODE>
DI void gemm_xw_tile(const Params& p, int panel, int ct, lds_ptr lds, Tag& tag) {
  constexpr int NT = MODE == 2 ? 3 : 5;
  constexpr unsigned LDW = MODE == 2 ? F_ * 2u : 3u * D_ * 2u;
  constexpr unsigned RB = NT * 64, WH = 32 * RB, NPIECE = WH / 1024, NPW = (NPIECE + 3) / 4;
  constexpr unsigned WSL = 32768, DUMP = WSL + 4 * 10240;
  const int tid = threadIdx.x, lane = tid & 63, w = __builtin_amdgcn_readfirstlane(tid >> 6);
  const int r32 = lane & 31, hh = lane >> 5;
  const int n0 = ct * NT * 32;
  const unsigned lds0 = (unsigned)(uintptr_t)lds;
  unsigned xv[2], wv[NPW];
#pragma unroll
  for (int i = 0; i < 2; ++i) {
    const int row = 8 * i + (lane >> 3);
    xv[i] = (unsigned)row * 1280u + (unsigned)(((lane & 7) ^ ((row >> 1) & 7)) * 16);
  }
  const unsigned xs0 = (unsigned)(panel * 128 + w * 32) * 1280u;
#pragma unroll
  for (int j = 0; j < (int)NPW; ++j) {
    const unsigned id = (unsigned)((j * 4 + w) * 64 + lane), row = id / (RB / 16), c = id - row * (RB / 16);
    wv[j] = id < WH / 16 ? row * LDW + c * 16u + (unsigned)n0 * 2u : OOB;
  }
  auto issue_x1 = [&](int st, int i) {
    dma16(mk_rsrc(p.x, st < 10 ? S_ * D_ * 2 : 0), xv[i & 1], xs0 + (unsigned)st * 128u + (unsigned)(i >> 1) * 20480u,
          lds0 + (unsigned)(st & 1) * 16384u + (unsigned)w * 4096u + (unsigned)i * 1024u);
  };
  auto issue_w1 = [&](int u, int j) {
    const unsigned pi = (unsigned)(j * 4 + w);
    dma16(mk_rsrc(MODE == 2 ? p.w1 : p.wqkv, u < 20 ? D_ * LDW : 0), wv[j], (unsigned)u * 32u * LDW,
          lds0 + (pi < NPIECE ? WSL + (unsigned)(u & 3) * WH + pi * 1024u : DUMP));
  };
  auto issue_x = [&](int st) {
#pragma unroll
    for (int i = 0; i < 4; ++i) issue_x1(st, i);
  };
  auto issue_w = [&](int u) {
#pragma unroll
    for (int j = 0; j < (int)NPW; ++j) issue_w1(u, j);
  };
  issue_x(0); issue_w(0); issue_w(1); issue_x(1); issue_w(2); issue_w(3);
  const rsrc_t rws = mk_rsrc(p.ws, WS_END);
  if (MODE != 2) {
    tag = load_tag(p);
    if (w == 0) flag_store(rws, SY_START + blockIdx.x * 8u, tag);
  }
  constexpr int XS = MODE != 2 ? 1 : 0;
  (void)XS;
  unsigned xa[4], wa;
  {
    const int row = w * 32 + r32, z = (row >> 1) & 7;
#pragma unroll
    for (int ks = 0; ks < 4; ++ks) xa[ks] = (unsigned)row * 128u + (unsigned)(((2 * ks + hh) ^ z) * 16);
    const int gq = lane >> 4, q = (lane & 15) >> 2, pp = lane & 3;
    wa = WSL + (unsigned)(8 * (gq >> 1) + q) * RB + (unsigned)((gq & 1) * 32 + pp * 8);
  }
  f32x16 acc[NT];
#pragma unroll
  for (int i = 0; i < NT; ++i) acc[i] = f32x16{};
  s16x8 xf[2][4], wf[2][2][NT];
  auto read_x = [&](int set) {
#pragma unroll
    for (int ks = 0; ks < 4; ++ks) xf[set][ks] = lds_b128(lds + xa[ks] + set * 16384);
  };
  auto read_w = [&](int set, int slot) {
#pragma unroll
    for (int k2 = 0; k2 < 2; ++k2)
#pragma unroll
      for (int i = 0; i < NT; ++i)
        wf[set][k2][i] = cat8(lds_tr(lds + wa + slot * WH + (16 * k2) * RB + i * 64), lds_tr(lds + wa + slot * WH + (16 * k2 + 4) * RB + i * 64));
  };
  const unsigned gt_off = SY_TOK + (unsigned)(panel * 128 + w * 32 + r32) * 16u;
  const unsigned gc_off = SY_COL + (unsigned)((MODE == 2 ? 1920 : 0) + n0 + r32) * 16u;
  u32x4 gt = u32x4{}, gc[NT];
#pragma unroll
  for (int i = 0; i < NT; ++i) gc[i] = u32x4{};
  auto load_stats = [&]() {
    gt = __builtin_amdgcn_raw_buffer_load_b128(rws, gt_off, 0, SC1);
#pragma unroll
    for (int i = 0; i < NT; ++i) gc[i] = __builtin_amdgcn_raw_buffer_load_b128(rws, gc_off, i * 512, SC1);
  };
  wait_bar<3 * NPW + 4>();
  read_x(0); read_w(0, 0);
  wait_bar<2 * NPW + 4>();
  auto step = [&](int u, auto Uc) {
    constexpr int U = decltype(Uc)::value;
    read_w((U + 1) & 1, (U + 1) & 3);
    if (U & 1) read_x(((U + 1) >> 1) & 1);
    SBAR();
#pragma unroll
    for (int k2 = 0; k2 < 2; ++k2) {
      const s16x8 xx = xf[U >> 1][(U & 1) * 2 + k2];
#pragma unroll
      for (int i = 0; i < NT; ++i) {
        acc[i] = MODE == 1 ? MFMA32(xx, wf[U & 1][k2][i], acc[i]) : MFMA32(wf[U & 1][k2][i], xx, acc[i]);
        const int m = k2 * NT + i;
        if (m < (int)NPW) issue_w1(u + 4, m);
        else if (!(U & 1) && m < (int)NPW + 4) issue_x1((u >> 1) + 2, m - NPW);
        SBAR();
      }
    }
    wait_bar<2 * NPW + 4>();
  };
  for (int u = 0; u < 20; u += 4) {
    if (u == 16) { load_stats(); if (MODE != 2 && w == 0) flag_store(rws, SY_GO + blockIdx.x * 8u, tag); }
    step(u, std::integral_constant<int, 0>{}); step(u + 1, std::integral_constant<int, 1>{});
    step(u + 2, std::integral_constant<int, 2>{}); step(u + 3, std::integral_constant<int, 3>{});
  }
  if (MODE == 2) __builtin_amdgcn_s_setprio(2);
  for (unsigned spin = 0; spin < SPIN_CAP; ++spin) {
    bool ok = gt[0] == tag.a && gt[2] == tag.b;
#pragma unroll
    for (int i = 0; i < NT; ++i) ok = ok && gc[i][0] == tag.a && gc[i][2] == tag.b;
    if (__all(ok)) break;
    __builtin_amdgcn_s_sleep(2);
    load_stats();
  }
  drain();
  const float mean = __uint_as_float(gt[1]);
  float rs = __uint_as_float(gt[3]);
  if (MODE == 2) rs *= __builtin_amdgcn_rsqf(__builtin_fmaf(-EPS * rs, rs, 1.0f + EPS));
  LAS float* st_a = (LAS float*)(lds + w * 4096);
  LAS float* st_b = (LAS float*)(lds + w * 4096 + 1024);
  if (MODE == 1) {
    if (hh == 0) { st_a[r32] = rs; st_b[r32] = mean * rs; }
  } else {
#pragma unroll
    for (int i = 0; i < NT; ++i) {
      if (hh == 0) st_a[i * 32 + r32] = __uint_as_float(gc[i][1]);
      else if (MODE == 2) st_b[i * 32 + r32] = bf_lo(p.b1[n0 + i * 32 + r32]);
    }
  }
  const unsigned tile = (unsigned)(panel * 4 + w);
  if constexpr (MODE == 0) {
    const int sect = ct >> 2;
    const float sc = sect == 0 ? rs * QSCALE : rs;
    const float ms = -mean;
    const unsigned hstride = sect == 0 ? 64u * 1280u : 64u * 2560u;
    const unsigned base = (sect == 0 ? WS_Q + tile * 1280u : WS_KV + (tile >> 1) * 5120u) + (unsigned)(ct & 3) * 8u * hstride;
    const unsigned o16 = base + (sect == 0 ? 0u : (tile & 1u) * 1024u) + (unsigned)hh * 512u + (unsigned)r32 * 16u;
    const unsigned o8 = base + (unsigned)hh * hstride + (sect == 0 ? 1024u + (unsigned)r32 * 8u : 2048u + (unsigned)r32 * 16u + (tile & 1u) * 8u);
    const float sm = sc * ms;
#pragma unroll
    for (int hp = 0; hp < 4; ++hp) {
      unsigned pk[10];
#pragma unroll
      for (int m = 0; m < 5; ++m) {
        const int sl = hp * 5 + m, i = sl >> 2, g = sl & 3;
        const f32x4 c4 = *(LAS const f32x4*)(st_a + i * 32 + 8 * g + 4 * hh);
        const f32x2 lo = (f32x2){acc[i][4 * g], acc[i][4 * g + 1]} * sc + (f32x2){c4[0], c4[1]} * sm;
        const f32x2 hi = (f32x2){acc[i][4 * g + 2], acc[i][4 * g + 3]} * sc + (f32x2){c4[2], c4[3]} * sm;
        pk[2 * m] = pack2(lo[0], lo[1]); pk[2 * m + 1] = pack2(hi[0], hi[1]);
      }
      __builtin_amdgcn_raw_buffer_store_b128((u32x4){pk[0], pk[1], pk[2], pk[3]}, rws, o16 + (2 * hp) * hstride, 0, SC1);
      __builtin_amdgcn_raw_buffer_store_b64((u32x2){pk[4], pk[5]}, rws, o8 + (2 * hp) * hstride, 0, SC1);
      __builtin_amdgcn_raw_buffer_store_b128((u32x4){pk[6], pk[7], pk[8], pk[9]}, rws, o16 + (2 * hp + 1) * hstride, 0, SC1);
      SBAR();
    }
    publish(rws, SY_FQ + ((unsigned)ct * 64u + tile) * 8u, tag, 0, 9);
  } else if constexpr (MODE == 1) {
    f32x4 r4[4], m4[4];
#pragma unroll
    for (int g = 0; g < 4; ++g) { r4[g] = *(LAS const f32x4*)(st_a + 8 * g + 4 * hh); m4[g] = *(LAS const f32x4*)(st_b + 8 * g + 4 * hh); }
#pragma unroll
    for (int i = 0; i < NT; ++i) {
      const float c = -__uint_as_float(gc[i][1]);
      const unsigned f = (unsigned)((ct & 3) * 160 + i * 32 + r32), head = f / 20u, d = f - head * 20u;
      float v[16];
#pragma unroll
      for (int g = 0; g < 4; ++g)
#pragma unroll
        for (int e = 0; e < 4; ++e) v[4 * g + e] = __builtin_fmaf(acc[i][4 * g + e], r4[g][e], m4[g][e] * c);
      const unsigned off = WS_KV + head * 163840u + (tile >> 1) * 5120u + 2560u + (tile & 1u) * 1280u + (unsigned)hh * 640u + d * 16u;
#pragma unroll
      for (int s = 0; s < 2; ++s)
        __builtin_amdgcn_raw_buffer_store_b128((u32x4){pack2(v[8 * s], v[8 * s + 1]), pack2(v[8 * s + 2], v[8 * s + 3]), pack2(v[8 * s + 4], v[8 * s + 5]), pack2(v[8 * s + 6], v[8 * s + 7])}, rws, off + s * 320, 0, SC1);
      SBAR();
    }
    publish(rws, SY_FQ + ((unsigned)ct * 64u + tile) * 8u, tag, 0, 9);
  } else {
    const float ms = -mean;
    const unsigned tok = (unsigned)(panel * 128 + w * 32 + r32);
    constexpr float G0 = -2.0f * 0.7978845608028654f * 1.4426950408889634f, G1 = G0 * 0.044715f;
    const unsigned off = WS_H + ((tok >> 6) * 192u + (unsigned)(n0 / 8 + hh)) * 1024u + (tok & 63u) * 16u;
#pragma unroll
    for (int i = 0; i < NT; ++i) {
      float v[16];
#pragma unroll
      for (int g = 0; g < 4; ++g) {
        const f32x4 c4 = *(LAS const f32x4*)(st_a + i * 32 + 8 * g + 4 * hh), b4 = *(LAS const f32x4*)(st_b + i * 32 + 8 * g + 4 * hh);
#pragma unroll
        for (int e = 0; e < 4; ++e) {
          const float u = __builtin_fmaf(rs, __builtin_fmaf(ms, c4[e], acc[i][4 * g + e]), b4[e]);
          v[4 * g + e] = sigmoid_mul(u, u * __builtin_fmaf(u * u, G1, G0));
        }
      }
#pragma unroll
      for (int s = 0; s < 2; ++s)
        __builtin_amdgcn_raw_buffer_store_b128((u32x4){pack2(v[8 * s], v[8 * s + 1]), pack2(v[8 * s + 2], v[8 * s + 3]), pack2(v[8 * s + 4], v[8 * s + 5]), pack2(v[8 * s + 6], v[8 * s + 7])}, rws, off + (i * 4 + 2 * s) * 1024, 0, SC1);
    }
    publish(rws, SY_FH + (tile * 16u + (unsigned)ct) * 8u, tag, 1, 9);
  }
}

DI void colsum_block(const Params& p, int e, lds_ptr lds, Tag tag) {
  const int tid = threadIdx.x, lane = tid & 63, w = tid >> 6;
  const bool isq = e < 30;
  const unsigned ldw = isq ? 3u * D_ : (unsigned)F_;
  const unsigned short* src = (isq ? p.wqkv + e * 64 : p.w1 + (e - 30) * 64) + (size_t)(tid >> 3) * 20u * ldw + (tid & 7) * 8;
  u32x4 v[20];
#pragma unroll
  for (int r = 0; r < 20; ++r) v[r] = *(const u32x4*)(src + (size_t)r * ldw);
  float s[8] = {0.f, 0.f, 0.f, 0.f, 0.f, 0.f, 0.f, 0.f};
#pragma unroll
  for (int r = 0; r < 20; ++r)
#pragma unroll
    for (int c = 0; c < 4; ++c) { s[2 * c] += bf_lo(v[r][c]); s[2 * c + 1] += bf_hi(v[r][c]); }
#pragma unroll
  for (int c = 0; c < 8; ++c) { s[c] += __shfl_xor(s[c], 8); s[c] += __shfl_xor(s[c], 16); s[c] += __shfl_xor(s[c], 32); }
  LAS float* red = (LAS float*)lds;
  if (lane < 8) {
#pragma unroll
    for (int c = 0; c < 8; ++c) red[w * 64 + lane * 8 + c] = s[c];
  }
  __syncthreads();
  if (tid < 64) {
    const unsigned t = __float_as_uint((red[tid] + red[64 + tid]) + (red[128 + tid] + red[192 + tid]));
    __builtin_amdgcn_raw_buffer_store_b128((u32x4){tag.a, t, tag.b, t}, mk_rsrc(p.ws, WS_END), SY_COL + (unsigned)(e * 64 + tid) * 16u, 0, SC1);
  }
}

DI void rowstat_block(const Params& p, int b, Tag& tag) {
  const int tid = threadIdx.x, r32 = tid & 31, row = b * 8 + (tid >> 5);
  const u32x2* src = (const u32x2*)(p.x + (size_t)row * D_) + r32;
  u32x2 v[5];
#pragma unroll
  for (int j = 0; j < 5; ++j) v[j] = src[j * 32];
  tag = load_tag(p);
  const rsrc_t rws = mk_rsrc(p.ws, WS_END);
  if (tid < 64) flag_store(rws, SY_START + blockIdx.x * 8u, tag);
  float f[20], s = 0.f;
#pragma unroll
  for (int j = 0; j < 5; ++j) { f[4 * j] = bf_lo(v[j][0]); f[4 * j + 1] = bf_hi(v[j][0]); f[4 * j + 2] = bf_lo(v[j][1]); f[4 * j + 3] = bf_hi(v[j][1]); }
#pragma unroll
  for (int j = 0; j < 20; ++j) s += f[j];
#pragma unroll
  for (int m = 1; m < 32; m <<= 1) s += __shfl_xor(s, m);
  const float mean = s * (1.0f / D_);
  float q = 0.f;
#pragma unroll
  for (int j = 0; j < 20; ++j) { const float d = f[j] - mean; q = __builtin_fmaf(d, d, q); }
#pragma unroll
  for (int m = 1; m < 32; m <<= 1) q += __shfl_xor(q, m);
  const float rs = __builtin_amdgcn_rsqf(q * (1.0f / D_) + EPS);
  if (r32 == 0) __builtin_amdgcn_raw_buffer_store_b128((u32x4){tag.a, __float_as_uint(mean), tag.b, __float_as_uint(rs)}, rws, SY_TOK + (unsigned)row * 16u, 0, SC1);
}

constexpr unsigned PAIR = 5456, SLOT = 4 * PAIR;

DI void attn_store_o(rsrc_t rws, const f32x16& o, float inv, unsigned tok, int head, int hh) {
  const unsigned base = WS_O + (tok >> 6) * 80u * 1024u + (tok & 63u) * 16u;
#pragma unroll
  for (int g = 0; g < 3; ++g) {
    const unsigned quad = (unsigned)(head * 5 + 2 * g + hh);
    const unsigned off = (g == 2 && hh) ? OOB : base + (quad >> 1) * 1024u + (quad & 1) * 8u;
    __builtin_amdgcn_raw_buffer_store_b64((u32x2){pack2(o[4 * g] * inv, o[4 * g + 1] * inv), pack2(o[4 * g + 2] * inv, o[4 * g + 3] * inv)}, rws, off, 0, SC1);
  }
}
#define PF(x) __builtin_bit_cast(s16x8, x)

DI f32x16 attn_safe(rsrc_t rws, int head, s16x8 q0, s16x8 q1, int r32, int hh) {
  f32x16 o = f32x16{};
  float m = -3.0e38f;
  const unsigned kb = WS_KV + (unsigned)head * 64u * 2560u;
  const u32x4 ones = (u32x4){0x3F803F80u, 0x3F803F80u, 0x3F803F80u, 0x3F803F80u};
  for (int t = 0; t < 64; ++t) {
    const unsigned tb = kb + (unsigned)(t >> 1) * 5120u, od = (unsigned)t & 1u;
    const u32x4 k0 = __builtin_amdgcn_raw_buffer_load_b128(rws, tb + od * 1024u + (unsigned)hh * 512u + (unsigned)r32 * 16u, 0, SC1);
    const u32x2 k1 = __builtin_amdgcn_raw_buffer_load_b64(rws, tb + 2048u + (unsigned)r32 * 16u + od * 8u, 0, SC1);
    const unsigned vo = r32 < 20 ? tb + 2560u + od * 1280u + (unsigned)hh * 640u + (unsigned)r32 * 16u : OOB;
    u32x4 v0 = __builtin_amdgcn_raw_buffer_load_b128(rws, vo, 0, SC1), v1 = __builtin_amdgcn_raw_buffer_load_b128(rws, vo, 320, SC1);
    if (r32 >= 20) { v0 = ones; v1 = ones; }
    f32x16 s = MFMA32(PF(k0), q0, f32x16{});
    s = MFMA32(PF(((u32x4){k1[0], k1[1], 0u, 0u})), q1, s);
    float mx = s[0];
#pragma unroll
    for (int r = 1; r < 16; ++r) mx = fmaxf(mx, s[r]);
    mx = fmaxf(mx, __shfl_xor(mx, 32));
    const float mn = fmaxf(m, mx), al = __builtin_amdgcn_exp2f(m - mn);
    m = mn;
#pragma unroll
    for (int r = 0; r < 16; ++r) { s[r] = __builtin_amdgcn_exp2f(s[r] - mn); o[r] *= al; }
    o = MFMA32(PF(v0), PF(((u32x4){pack2(s[0], s[1]), pack2(s[2], s[3]), pack2(s[4], s[5]), pack2(s[6], s[7])})), o);
    o = MFMA32(PF(v1), PF(((u32x4){pack2(s[8], s[9]), pack2(s[10], s[11]), pack2(s[12], s[13]), pack2(s[14], s[15])})), o);
  }
  return o;
}
DI bool attn_unsafe(const f32x16& o, float l) {
  float mx = 0.f;
#pragma unroll
  for (int r = 0; r < 12; ++r) mx = fmaxf(mx, fabsf(o[r]));
  bool nan = false;
#pragma unroll
  for (int r = 0; r < 12; ++r) nan = nan || (o[r] != o[r]);
  return !(l > 9.0e-13f && l < 1.0e12f) || !(mx < 1.0e36f) || nan;
}

DI void attn_block(const Params& p, int head, int qt, lds_ptr lds, Tag tag) {
  const int tid = threadIdx.x, lane = tid & 63, w = __builtin_amdgcn_readfirstlane(tid >> 6);
  const int r32 = lane & 31, hh = lane >> 5;
  const unsigned lds0 = (unsigned)(uintptr_t)lds;
  const rsrc_t rws = mk_rsrc(p.ws, WS_END);
  if (w == 0) {
    const unsigned c = (unsigned)head >> 3;
    const unsigned off[3] = {SY_FQ + ((4 + c) * 64u + (unsigned)lane) * 8u, SY_FQ + ((8 + c) * 64u + (unsigned)lane) * 8u,
                             lane < 8 ? SY_FQ + (c * 64u + (unsigned)(qt * 8 + lane)) * 8u : OOB};
    poll_flags<3>(rws, off, tag);
  }
  wait_bar<0>();
  const unsigned kv0 = WS_KV + (unsigned)head * 64u * 2560u + (unsigned)w * 5120u;
  {
    const unsigned qb = WS_Q + ((unsigned)head * 64u + (unsigned)(qt * 8 + w * 2)) * 1280u;
#pragma unroll
    for (int i = 0; i < 3; ++i) dma16_sc1(rws, (unsigned)lane * 16u, qb + (unsigned)i * 1024u, lds0 + 3 * SLOT + (unsigned)w * 3072u + (unsigned)i * 1024u);
  }
#pragma unroll
  for (int i = 0; i < 9; ++i)
    dma16_sc1(rws, (unsigned)lane * 16u, kv0 + (unsigned)(i / 5) * 20480u + (unsigned)(i % 5) * 1024u, lds0 + (unsigned)(i / 5) * SLOT + (unsigned)w * PAIR + (unsigned)(i % 5) * 1024u);
  if (tid < 24) *(LAS u32x4*)(lds + (tid >> 1) * PAIR + 5120 + (tid & 1) * 320) = (u32x4){0x3F803F80u, 0x3F803F80u, 0x3F803F80u, 0x3F803F80u};
  s16x8 qf0[2], qe[2], qo[2];
  const unsigned ka0 = (unsigned)hh * 512u + (unsigned)r32 * 16u, ka1 = 2048u + (unsigned)r32 * 16u;
  const unsigned vaE = r32 < 20 ? (unsigned)hh * 640u + (unsigned)r32 * 16u : 2560u;
  const unsigned vaO = r32 < 20 ? vaE : 1280u;
  f32x16 oa = f32x16{}, ob = f32x16{};
  f32x16 sA0, sA1, sB0, sB1;
  s16x8 k0[2], k1[2], v0[2], v1[2];
  u32x4 pa0, pa1, pb0, pb1;
#define RD_K(slot, i) do { k0[(i) & 1] = lds_b128(lds + ka0 + (slot) + ((i) >> 1) * PAIR + ((i) & 1) * 1024); \
    if (!((i) & 1)) k1[((i) >> 1) & 1] = lds_b128(lds + ka1 + (slot) + ((i) >> 1) * PAIR); } while (0)
#define RD_V(slot, i) do { v0[(i) & 1] = lds_b128(lds + (((i) & 1) ? vaO : vaE) + (slot) + ((i) >> 1) * PAIR + 2560 + ((i) & 1) * 1280); \
    v1[(i) & 1] = lds_b128(lds + (((i) & 1) ? vaO : vaE) + (slot) + ((i) >> 1) * PAIR + 2880 + ((i) & 1) * 1280); } while (0)
#define EX4(S, r) do { S[r] = __builtin_amdgcn_exp2f(S[r]); S[r + 1] = __builtin_amdgcn_exp2f(S[r + 1]); S[r + 2] = __builtin_amdgcn_exp2f(S[r + 2]); S[r + 3] = __builtin_amdgcn_exp2f(S[r + 3]); \
    SBAR(); } while (0)
#define CV2(P, j, S, r) do { P[j] = pack2(S[r], S[r + 1]); P[j + 1] = pack2(S[r + 2], S[r + 3]); SBAR(); } while (0)
  const f32x16 zero = f32x16{};
  wait_bar<4>();
#pragma unroll
  for (int a = 0; a < 2; ++a) {
    qf0[a] = lds_b128(lds + 3 * SLOT + w * 3072 + a * 1280 + ka0);
    const u32x2 t = *(LAS const u32x2*)(lds + 3 * SLOT + w * 3072 + a * 1280 + 1024 + r32 * 8);
    const unsigned t0 = hh ? 0u : t[0], t1 = hh ? 0u : t[1];
    qe[a] = PF(((u32x4){t0, t1, 0u, 0u})); qo[a] = PF(((u32x4){0u, 0u, t0, t1}));
  }
  RD_K(0, 0); RD_K(0, 1); RD_V(0, 0);
  __builtin_amdgcn_s_setprio(1);
  sA0 = MFMA32(k0[0], qf0[0], zero); sA0 = MFMA32(k1[0], qe[0], sA0);
  sA1 = MFMA32(k0[0], qf0[1], zero); sA1 = MFMA32(k1[0], qe[1], sA1);
  pb1 = u32x4{}; sB1 = zero; v1[1] = v0[0];
#define Q1(T, a) ((((T) + 1) & 1) ? qo[a] : qe[a])
#define STEP(C0, C1, N0, N1, T, KS, VS, D0, D1) do { \
    __builtin_amdgcn_s_waitcnt(0xC07F); SBAR(); \
    RD_K(KS, ((T) + 2) & 7); SBAR(); \
    N0 = MFMA32(k0[((T) + 1) & 1], qf0[0], zero); SBAR();                  D0; CV2(pb1, 2, N1, 12); EX4(C0, 0); \
    ob = MFMA32(v1[((T) + 1) & 1], PF(pb1), ob); SBAR();                   RD_V(VS, ((T) + 1) & 7); SBAR(); CV2(pa0, 0, C0, 0); EX4(C0, 4); \
    N0 = MFMA32(k1[(((T) + 1) >> 1) & 1], Q1(T, 0), N0); SBAR();           CV2(pa0, 2, C0, 4); EX4(C0, 8); \
    oa = MFMA32(v0[(T) & 1], PF(pa0), oa); SBAR();                         CV2(pa1, 0, C0, 8); EX4(C0, 12); \
    N1 = MFMA32(k0[((T) + 1) & 1], qf0[1], zero); SBAR();                  D1; CV2(pa1, 2, C0, 12); EX4(C1, 0); \
    oa = MFMA32(v1[(T) & 1], PF(pa1), oa); SBAR();                         CV2(pb0, 0, C1, 0); EX4(C1, 4); \
    N1 = MFMA32(k1[(((T) + 1) >> 1) & 1], Q1(T, 1), N1); SBAR();           CV2(pb0, 2, C1, 4); EX4(C1, 8); \
    ob = MFMA32(v0[(T) & 1], PF(pb0), ob); SBAR();                         CV2(pb1, 0, C1, 8); EX4(C1, 12); \
  } while (0)
#define KVP(cc, sl, i) do { dma16_sc1(mk_rsrc(p.ws, (cc) < 8 ? WS_END : 0), (unsigned)lane * 16u, kv0 + (unsigned)(cc) * 20480u + (i) * 1024u, lds0 + (sl) + (unsigned)w * PAIR + (i) * 1024u); SBAR(); } while (0)
#define NOP_ do { } while (0)
  unsigned cur = 0, nxt = SLOT, prv = 2 * SLOT;
  for (int c = 0; c < 8; ++c) {
    STEP(sA0, sA1, sB0, sB1, 0, cur, cur, KVP(c + 1, nxt, 4), NOP_);
    STEP(sB0, sB1, sA0, sA1, 1, cur, cur, NOP_, NOP_);
    STEP(sA0, sA1, sB0, sB1, 2, cur, cur, NOP_, NOP_);
    STEP(sB0, sB1, sA0, sA1, 3, cur, cur, NOP_, NOP_);
    STEP(sA0, sA1, sB0, sB1, 4, cur, cur, NOP_, NOP_);
    STEP(sB0, sB1, sA0, sA1, 5, cur, cur, NOP_, NOP_);
    wait_bar<0>();
    STEP(sA0, sA1, sB0, sB1, 6, nxt, cur, KVP(c + 2, prv, 0), KVP(c + 2, prv, 1));
    STEP(sB0, sB1, sA0, sA1, 7, nxt, nxt, KVP(c + 2, prv, 2), KVP(c + 2, prv, 3));
    const unsigned t_ = cur; cur = nxt; nxt = prv; prv = t_;
  }
  __builtin_amdgcn_s_setprio(3);
  CV2(pb1, 2, sB1, 12);
  ob = MFMA32(v1[1], PF(pb1), ob);
  float la = hh ? oa[8] : oa[12], lb = hh ? ob[8] : ob[12];
  if (__builtin_expect(__any(attn_unsafe(oa, la) || attn_unsafe(ob, lb)), 0)) {
    oa = attn_safe(rws, head, qf0[0], qe[0], r32, hh);
    ob = attn_safe(rws, head, qf0[1], qe[1], r32, hh);
    la = hh ? oa[8] : oa[12]; lb = hh ? ob[8] : ob[12];
  }
  const unsigned tok = (unsigned)(qt * 256 + w * 64 + r32);
  attn_store_o(rws, oa, __builtin_amdgcn_rcpf(la), tok, head, hh);
  attn_store_o(rws, ob, __builtin_amdgcn_rcpf(lb), tok + 32, head, hh);
  publish(rws, SY_FO + ((unsigned)(qt * 4 + w) * 32u + (unsigned)head) * 8u, tag, 0, 10);
}

constexpr unsigned ST2 = 20480;
DI void tail_block(const Params& p, int rt, int cn, lds_ptr lds, Tag tag) {
  const int tid = threadIdx.x, lane = tid & 63, w = __builtin_amdgcn_readfirstlane(tid >> 6);
  const unsigned lds0 = (unsigned)(uintptr_t)lds;
  const rsrc_t rws = mk_rsrc(p.ws, WS_END);
  const unsigned n0 = (unsigned)cn * 80u;
  const int g = lane >> 4, q = (lane & 15) >> 2, pp = lane & 3;
  const unsigned afrag = (unsigned)g * 1024u + (unsigned)(w * 16 + (lane & 15)) * 16u;
  if (w == 0) {
    const unsigned off[1] = {lane < 32 ? SY_FH + ((unsigned)rt * 32u + (unsigned)lane) * 8u : OOB};
    poll_flags<1>(rws, off, tag);
  }
  wait_bar<0>();
  __builtin_amdgcn_s_setprio(0);
  f32x4 am[5], as[5];
#pragma unroll
  for (int j = 0; j < 5; ++j) { am[j] = f32x4{}; as[j] = f32x4{}; }
  {
    unsigned wv[3];
#pragma unroll
    for (int i = 0; i < 3; ++i) {
      const int id = i * 64 + lane, row = id / 10, c = id - row * 10;
      wv[i] = id < 160 ? (unsigned)row * 1280u + (unsigned)c * 16u + n0 * 2u : OOB;
    }
    const unsigned a0 = WS_H + (unsigned)rt * 192u * 1024u;
    auto issue = [&](int st, unsigned slot) {
      const bool ok = st < 24;
      const rsrc_t ra = mk_rsrc(p.ws, ok ? WS_END : 0), rw = mk_rsrc(p.w2, ok ? F_ * D_ * 2 : 0);
      dma16_sc1(ra, afrag, a0 + (unsigned)st * 8192u, lds0 + slot + (unsigned)w * 2048u);
      dma16_sc1(ra, afrag, a0 + (unsigned)st * 8192u + 4096u, lds0 + slot + (unsigned)w * 2048u + 1024u);
#pragma unroll
      for (int i = 0; i < 3; ++i) dma16(rw, wv[i], (unsigned)(st * 64 + w * 16) * 1280u, lds0 + slot + 8192u + (unsigned)w * 3072u + (unsigned)i * 1024u);
    };
    issue(0, 0); issue(1, ST2); issue(2, 2 * ST2);
    const unsigned aa = (unsigned)w * 2048u + (unsigned)lane * 16u;
    const unsigned wa = 8192u + (unsigned)(g >> 1) * 3072u + (unsigned)(4 * (g & 1) + q) * 160u + (unsigned)pp * 8u;
    unsigned slot = 0, fill = 3 * ST2;
    for (int st = 0; st < 24; ++st) {
      wait_bar<10>();
      s16x8 af[2], wf[2][5];
#pragma unroll
      for (int s = 0; s < 2; ++s) {
        af[s] = lds_b128(lds + aa + slot + s * 1024);
#pragma unroll
        for (int j = 0; j < 5; ++j) wf[s][j] = cat8(lds_tr(lds + wa + slot + s * 6144 + j * 32), lds_tr(lds + wa + slot + s * 6144 + j * 32 + 1280));
      }
      SBAR();
      issue(st + 3, fill);
      SBAR();
#pragma unroll
      for (int s = 0; s < 2; ++s)
#pragma unroll
        for (int j = 0; j < 5; ++j) am[j] = MFMA16(wf[s][j], af[s], am[j]);
      fill = slot; slot = slot == 3 * ST2 ? 0 : slot + ST2;
    }
    wait_bar<0>();
  }
  __builtin_amdgcn_s_setprio(2);
  const rsrc_t rwo = mk_rsrc(p.wo, D_ * D_ * 2);
  unsigned ov[5];
#pragma unroll
  for (int i = 0; i < 5; ++i) {
    const int id = i * 64 + lane, row = id / 10, c = id - row * 10;
    ov[i] = (unsigned)row * 1280u + (unsigned)c * 16u + n0 * 2u;
  }
  const unsigned woa = (unsigned)(8 * g + q) * 160u + (unsigned)pp * 8u;
#pragma unroll
  for (int i = 0; i < 5; ++i) dma16(rwo, ov[i], (unsigned)(512 + w * 32) * 1280u, lds0 + (unsigned)w * 5120u + (unsigned)i * 1024u);
  const unsigned tok = (unsigned)(rt * 64 + w * 16 + (lane & 15));
  u32x2 xb[5], bb[5];
#pragma unroll
  for (int j = 0; j < 5; ++j) { xb[j] = *(const u32x2*)(p.x + tok * D_ + n0 + j * 16 + g * 4); bb[j] = *(const u32x2*)(p.b2 + n0 + j * 16 + g * 4); }
  wait_bar<0>();
  s16x8 wr[4][5];
#pragma unroll
  for (int s = 0; s < 4; ++s)
#pragma unroll
    for (int j = 0; j < 5; ++j) wr[s][j] = cat8(lds_tr(lds + woa + s * 5120 + j * 32), lds_tr(lds + woa + s * 5120 + j * 32 + 640));
#pragma unroll
  for (int j = 0; j < 5; ++j) {
#pragma unroll
    for (int e = 0; e < 4; ++e) {
      const float m = am[j][e] + (e & 1 ? bf_hi(bb[j][e >> 1]) : bf_lo(bb[j][e >> 1]));
      am[j][e] = sigmoid_mul(m, m * -1.4426950408889634f);
    }
    asm volatile("" : "+v"(am[j]), "+v"(xb[j]));
  }
  wait_bar<0>();
#pragma unroll
  for (int r = 0; r < 4; ++r)
#pragma unroll
    for (int i = 0; i < 5; ++i) dma16(rwo, ov[i], (unsigned)((r * 4 + w) * 32) * 1280u, lds0 + (unsigned)(r * 4 + w) * 5120u + (unsigned)i * 1024u);
  if (w == 0) {
    const unsigned off[1] = {lane < 32 ? SY_FO + ((unsigned)rt * 32u + (unsigned)lane) * 8u : OOB};
    poll_flags<1>(rws, off, tag);
  }
  wait_bar<0>();
  u32x4 of[20];
#pragma unroll
  for (int s = 0; s < 20; ++s) of[s] = __builtin_amdgcn_raw_buffer_load_b128(rws, afrag, WS_O + (unsigned)rt * 81920u + (unsigned)s * 4096u, SC1);
#pragma unroll
  for (int s = 0; s < 20; ++s)
#pragma unroll
    for (int j = 0; j < 5; ++j) {
      const s16x8 wf = s < 16 ? cat8(lds_tr(lds + woa + s * 5120 + j * 32), lds_tr(lds + woa + s * 5120 + j * 32 + 640)) : wr[s < 16 ? 0 : s - 16][j];
      as[j] = MFMA16(wf, PF(of[s]), as[j]);
    }
#pragma unroll
  for (int j = 0; j < 5; ++j) {
    float r[4];
#pragma unroll
    for (int e = 0; e < 4; ++e) r[e] = __builtin_fmaf(as[j][e], am[j][e], e & 1 ? bf_hi(xb[j][e >> 1]) : bf_lo(xb[j][e >> 1]));
    *(u32x2*)(p.out + tok * D_ + n0 + j * 16 + g * 4) = (u32x2){pack2(r[0], r[1]), pack2(r[2], r[3])};
  }
}

__global__ __launch_bounds__(256, 2) void fused_block(Params p) {
  extern __shared__ __attribute__((aligned(16))) char smem[];
  lds_ptr lds = (lds_ptr)smem;
  const int b = blockIdx.x & 255, xcd = b & 7, j = b >> 3;
  const int panel = (xcd & 1) * 8 + (j & 7);
  Tag tag;
  if (blockIdx.x < 256) {
    __builtin_amdgcn_s_setprio(3);
    if (j < 24) {
      const int c = (xcd >> 1) * 3 + (j >> 3);
      if (c < 8) gemm_xw_tile<0>(p, panel, c, lds, tag);
      else gemm_xw_tile<1>(p, panel, c, lds, tag);
    } else {
      const int e = xcd * 8 + (j - 24);
      const rsrc_t rws = mk_rsrc(p.ws, WS_END);
      tag = load_tag(p);
      if (threadIdx.x < 64) flag_store(rws, SY_START + blockIdx.x * 8u, tag);
      if (e < 54) colsum_block(p, e, lds, tag);
      else if (e == 63 && threadIdx.x < 64) {
        unsigned off[8];
#pragma unroll
        for (int i = 0; i < 8; ++i) off[i] = SY_START + (unsigned)(i * 64 + (int)threadIdx.x) * 8u;
        poll_flags<8>(rws, off, tag);
        flag_store(rws, SY_GEN, tag);
      }
    }
    attn_block(p, xcd * 4 + (j >> 3), j & 7, lds, tag);
  } else {
    __builtin_amdgcn_s_setprio(2);
    rowstat_block(p, b, tag);
    if (j < 24) {
      if (threadIdx.x < 64) {
        const unsigned off[1] = {threadIdx.x == 0 ? SY_GO + (unsigned)b * 8u : OOB};
        poll_flags<1>(mk_rsrc(p.ws, WS_END), off, tag);
      }
      wait_bar<0>();
    }
    __builtin_amdgcn_s_setprio(0);
    gemm_xw_tile<2>(p, panel, (xcd >> 1) * 4 + (j >> 3), lds, tag);
    tail_block(p, (xcd >> 1) * 8 + (j & 7), (xcd & 1) * 4 + (j >> 3), lds, tag);
  }
}

extern "C" void kernel_launch(void* const* d_in, const int* in_sizes, int n_in, void* d_out, int out_size, void* d_ws, size_t ws_size, hipStream_t stream) {
  Params p;
  p.x = (const unsigned short*)d_in[0];
  p.wqkv = (const unsigned short*)d_in[1];
  p.wo = (const unsigned short*)d_in[2];
  p.w1 = (const unsigned short*)d_in[4];
  p.b1 = (const unsigned short*)d_in[5];
  p.w2 = (const unsigned short*)d_in[6];
  p.b2 = (const unsigned short*)d_in[7];
  p.out = (unsigned short*)d_out;
  p.ws = (char*)d_ws;
  (void)hipFuncSetAttribute((const void*)fused_block, hipFuncAttributeMaxDynamicSharedMemorySize, LDS_BYTES);
  fused_block<<<512, 256, LDS_BYTES, stream>>>(p);
}
```

```cpp
#include <hip/hip_runtime.h>
#include <stdint.h>
#include <type_traits>

typedef __attribute__((ext_vector_type(8))) short s16x8;
typedef __attribute__((ext_vector_type(4))) short s16x4;
typedef __attribute__((ext_vector_type(16))) float f32x16;
typedef __attribute__((ext_vector_type(4))) float f32x4;
typedef __attribute__((ext_vector_type(2))) float f32x2;
typedef __attribute__((ext_vector_type(4))) unsigned u32x4;
typedef __attribute__((ext_vector_type(2))) unsigned u32x2;
typedef __attribute__((ext_vector_type(2))) __bf16 bf16x2;
#define LAS __attribute__((address_space(3)))
#define DI __device__ __forceinline__
typedef LAS char* lds_ptr;
typedef __amdgpu_buffer_rsrc_t rsrc_t;
template <int I, int N, typename F> DI void static_for(F&& f) {
  if constexpr (I < N) { f(std::integral_constant<int, I>{}); static_for<I + 1, N>(f); }
}

constexpr int S_ = 2048, D_ = 640, F_ = 1536;
constexpr float EPS = 1e-5f;
constexpr float QSCALE = 0.22360679774997896f * 1.4426950408889634f;
constexpr int SC1 = 16;
constexpr unsigned OOB = 0x80000000u;

constexpr unsigned WS_Q = 0;
constexpr unsigned WS_KV = WS_Q + 32u * 64u * 1280u;
constexpr unsigned WS_H = WS_KV + 32u * 64u * 2560u;
constexpr unsigned WS_O = WS_H + 2048u * 1536u * 2u;
constexpr unsigned WS_W2 = WS_O + 2048u * 640u * 2u;
constexpr unsigned WS_WO = WS_W2 + 1536u * 640u * 2u;
constexpr unsigned SY_GEN = WS_WO + 640u * 640u * 2u;
constexpr unsigned SY_START = SY_GEN + 256;
constexpr unsigned SY_GO = SY_START + 512 * 8;
constexpr unsigned SY_FQ = SY_GO + 256 * 128;
constexpr unsigned SY_FH = SY_FQ + 16 * 64 * 8;
constexpr unsigned SY_FO = SY_FH + 64 * 16 * 8;
constexpr unsigned SY_FW = SY_FO + 32 * 32 * 8;
constexpr unsigned SY_TOK = SY_FW + 8 * 96 * 8;
constexpr unsigned SY_COL = SY_TOK + 2048 * 16;
constexpr unsigned WS_END = SY_COL + 3456 * 16;

constexpr int LDS_BYTES = 81920;
constexpr unsigned SPIN_CAP = 1u << 22;

DI unsigned pack2(float a, float b) { return __builtin_bit_cast(unsigned, __builtin_convertvector((f32x2){a, b}, bf16x2)); }
DI float bf_lo(unsigned v) { return __uint_as_float(v << 16); }
DI float bf_hi(unsigned v) { return __uint_as_float(v & 0xFFFF0000u); }
DI rsrc_t mk_rsrc(const void* p, unsigned bytes) { return __builtin_amdgcn_make_buffer_rsrc((void*)p, (short)0, (int)bytes, 0x00020000); }
DI void dma16(rsrc_t rs, unsigned voff, unsigned soff, unsigned ldsaddr) {
  asm volatile("s_mov_b32 m0, %0\n\ts_nop 0\n\tbuffer_load_dwordx4 %1, %2, %3 offen lds" ::"s"(ldsaddr), "v"(voff), "s"(rs), "s"(soff) : "memory");
}
DI void dma16_sc1(rsrc_t rs, unsigned voff, unsigned soff, unsigned ldsaddr) {
  asm volatile("s_mov_b32 m0, %0\n\ts_nop 0\n\tbuffer_load_dwordx4 %1, %2, %3 offen sc1 lds" ::"s"(ldsaddr), "v"(voff), "s"(rs), "s"(soff) : "memory");
}
DI void dma16_half(rsrc_t rs, unsigned voff, unsigned soff, unsigned ldsaddr) {
  asm volatile("s_mov_b32 m0, %0\n\ts_mov_b64 exec, 0xffffffff\n\tbuffer_load_dwordx4 %1, %2, %3 offen lds\n\ts_mov_b64 exec, -1" ::"s"(ldsaddr), "v"(voff), "s"(rs), "s"(soff) : "memory");
}
DI void dma16_half_sc1(rsrc_t rs, unsigned voff, unsigned soff, unsigned ldsaddr) {
  asm volatile("s_mov_b32 m0, %0\n\ts_mov_b64 exec, 0xffffffff\n\tbuffer_load_dwordx4 %1, %2, %3 offen sc1 lds\n\ts_mov_b64 exec, -1" ::"s"(ldsaddr), "v"(voff), "s"(rs), "s"(soff) : "memory");
}
template <int N> DI void wait_bar() { asm volatile("s_waitcnt vmcnt(%0) lgkmcnt(0)\n\ts_barrier" ::"i"(N) : "memory"); }
DI void drain() { asm volatile("s_waitcnt vmcnt(0)" ::: "memory"); }
#define SBAR() __builtin_amdgcn_sched_barrier(0)
#define MFMA32(a, b, c) __builtin_amdgcn_mfma_f32_32x32x16_bf16((a), (b), (c), 0, 0, 0)
#define MFMA16(a, b, c) __builtin_amdgcn_mfma_f32_16x16x32_bf16((a), (b), (c), 0, 0, 0)
DI s16x8 lds_b128(lds_ptr p) { return *(LAS const s16x8*)p; }
DI s16x4 lds_tr(lds_ptr p) { return __builtin_amdgcn_ds_read_tr16_b64_v4i16((LAS s16x4*)p); }
DI s16x8 cat8(s16x4 a, s16x4 b) { return __builtin_shufflevector(a, b, 0, 1, 2, 3, 4, 5, 6, 7); }
DI float sigmoid_mul(float u, float w  ) { return u * __builtin_amdgcn_rcpf(1.0f + __builtin_amdgcn_exp2f(w)); }

struct Params {
  const unsigned short* x;
  const unsigned short* wqkv;
  const unsigned short* wo;
  const unsigned short* w1;
  const unsigned short* b1;
  const unsigned short* w2;
  const unsigned short* b2;
  unsigned short* out;
  char* ws;
};
struct Tag { unsigned a, b; };

DI Tag load_tag(const Params& p) {
  unsigned long long g;
  asm volatile("s_load_dwordx2 %0, %1, 0x0\n\ts_waitcnt lgkmcnt(0)" : "=s"(g) : "s"(p.ws + SY_GEN) : "memory");
  g += 0x9E3779B97F4A7C15ull; g = (g ^ (g >> 30)) * 0xBF58476D1CE4E5B9ull; g = (g ^ (g >> 27)) * 0x94D049BB133111EBull; g ^= g >> 31;
  return Tag{(unsigned)g, (unsigned)(g >> 32)};
}
DI void flag_store(rsrc_t rws, unsigned off, Tag t) {
  if ((threadIdx.x & 63) == 0) __builtin_amdgcn_raw_buffer_store_b64((u32x2){t.a, t.b}, rws, off, 0, SC1);
}
DI void publish(rsrc_t rws, unsigned off, Tag t) { drain(); flag_store(rws, off, t); }
template <int N, int SLEEP = 2> DI void poll_flags(rsrc_t rws, const unsigned (&off)[N], Tag t) {
  for (unsigned spin = 0; spin < SPIN_CAP; ++spin) {
    u32x2 v[N];
#pragma unroll
    for (int i = 0; i < N; ++i) v[i] = __builtin_amdgcn_raw_buffer_load_b64(rws, off[i], 0, SC1);
    unsigned bad = 0;
#pragma unroll
    for (int i = 0; i < N; ++i) bad |= off[i] == OOB ? 0u : ((v[i][0] ^ t.a) | (v[i][1] ^ t.b));
    const bool ok = bad == 0;
    if (__all(ok)) break;
    __builtin_amdgcn_s_sleep(SLEEP);
  }
  asm volatile("" ::: "memory");
}

template <int MODE>
DI void gemm_xw_tile(const Params& p, int panel, int ct, lds_ptr lds, Tag& tag) {
  constexpr int NT = MODE == 2 ? 3 : 4;
  constexpr unsigned LDW = MODE == 2 ? F_ * 2u : 3u * D_ * 2u;
  constexpr unsigned RB = NT * 64, WH = 32 * RB, WSL = 49152;
  constexpr int W2 = 4;
  const int tid = threadIdx.x, lane = tid & 63, w = __builtin_amdgcn_readfirstlane(tid >> 6);
  const int r32 = lane & 31, hh = lane >> 5;
  const int n0 = ct * NT * 32;
  const unsigned lds0 = (unsigned)(uintptr_t)lds;
  unsigned xv[2], wv[2];
#pragma unroll
  for (int i = 0; i < 2; ++i) {
    const int row = 8 * i + (lane >> 3);
    xv[i] = (unsigned)row * 1280u + (unsigned)(((lane & 7) ^ ((row >> 1) & 7)) * 16);
  }
  const unsigned xs0 = (unsigned)(panel * 128 + w * 32) * 1280u;
  const unsigned wd1 = NT == 4 ? 4096u + (unsigned)w * 1024u : 4096u + (unsigned)w * 512u;
#pragma unroll
  for (int j = 0; j < 2; ++j) {
    const unsigned id = (j == 0 ? (unsigned)w * 1024u : wd1) / 16u + (unsigned)lane;
    if (NT == 4) {
      const unsigned row = id >> 4, c = (id & 15u) ^ ((row & 3u) << 2);
      wv[j] = row * LDW + c * 16u + (unsigned)n0 * 2u;
    } else {
      const unsigned row = id / 12u, c = id - row * 12u;
      wv[j] = row * LDW + c * 16u + (unsigned)n0 * 2u;
    }
  }
  const rsrc_t rws = mk_rsrc(p.ws, WS_END);
  unsigned goff[3];
  auto issue_x1 = [&](int st, int i) {
    const unsigned dst = lds0 + (unsigned)(st % 3) * 16384u + (unsigned)w * 4096u + (unsigned)i * 1024u;
    if (st < 10) dma16(mk_rsrc(p.x, S_ * D_ * 2), xv[i & 1], xs0 + (unsigned)st * 128u + (unsigned)(i >> 1) * 20480u, dst);
    else if (st == 11 && i < 3) dma16_sc1(rws, goff[i < 3 ? i : 0], 0u, dst);
  };
  auto issue_w1 = [&](int u, int j) {
    if (u >= 20) return;
    const rsrc_t rw = mk_rsrc(MODE == 2 ? p.w1 : p.wqkv, D_ * LDW);
    const unsigned dst = lds0 + WSL + (unsigned)(u & 3) * WH + (j == 0 ? (unsigned)w * 1024u : wd1);
    if (NT == 3 && j == 1) dma16_half(rw, wv[j], (unsigned)u * 32u * LDW, dst);
    else dma16(rw, wv[j], (unsigned)u * 32u * LDW, dst);
  };
  auto issue_x = [&](int st) {
#pragma unroll
    for (int i = 0; i < 4; ++i) issue_x1(st, i);
  };
  auto issue_w = [&](int u) { issue_w1(u, 0); issue_w1(u, 1); };
  if (MODE == 2 && ct < 15) {
    for (int i = 0; i < 4; ++i) __builtin_amdgcn_s_sleep(16);
  }
  issue_x(0); issue_w(0); issue_w(1); issue_x(1); issue_w(2); issue_w(3); issue_x(2);
  if (MODE != 2) {
    tag = load_tag(p);
    if (w == 0) flag_store(rws, SY_START + blockIdx.x * 8u, tag);
  } else if (ct < 15 && w == 0) {
    const unsigned off[1] = {lane == 0 ? SY_GO + (blockIdx.x & 255u) * 128u : OOB};
    poll_flags<1, 8>(rws, off, tag);
  }
  const int c5 = ct % 5, od = c5 & 1, H0 = (32 * c5 + (od ? 0 : 4)) / 5;
  const unsigned long long singles = c5 == 0 ? 0x1F1E1D18130E0904ull : c5 == 1 ? 0x1F1E1B16110C0702ull : c5 == 2 ? 0x1F1E19140F0A0500ull
                                   : c5 == 3 ? 0x1F1C17120D080300ull : 0x1F1A15100B060100ull;
  auto quad_of = [&](int i, int g, int h) -> int {
    if (MODE != 0) return 8 * i + 2 * g + h;
    if (i == 3) return (int)((singles >> (8 * (2 * g + h))) & 31ull);
    const int kk = (2 * i + (g >> 1)) * 2 + h + od;
    return 5 * (H0 + (kk >> 1)) + 2 * (kk & 1) + (g & 1) - 32 * c5;
  };
  unsigned xa[4], wa[NT], gc_off[NT];
  {
    const int row = w * 32 + r32, z = (row >> 1) & 7;
#pragma unroll
    for (int ks = 0; ks < 4; ++ks) xa[ks] = (unsigned)row * 128u + (unsigned)(((2 * ks + hh) ^ z) * 16);
    const int gq = lane >> 4, q = (lane & 15) >> 2, pp = lane & 3;
#pragma unroll
    for (int i = 0; i < NT; ++i) {
      const unsigned col = (unsigned)quad_of(i, 2 * (gq & 1) + (pp >> 1), pp & 1) * 8u;
      wa[i] = WSL + (unsigned)(8 * (gq >> 1) + q) * RB + (NT == 4 ? col ^ ((unsigned)q << 6) : col);
      gc_off[i] = SY_COL + (unsigned)((MODE == 2 ? 1920 : 0) + n0 + 4 * quad_of(i, r32 >> 3, (r32 >> 2) & 1) + (r32 & 3)) * 16u;
    }
  }
  f32x16 acc[NT];
#pragma unroll
  for (int i = 0; i < NT; ++i) acc[i] = f32x16{};
  s16x8 xf[2][4], wf[2][2][NT];
  auto read_x = [&](int set, unsigned slot) {
#pragma unroll
    for (int ks = 0; ks < 4; ++ks) xf[set][ks] = lds_b128(lds + xa[ks] + slot);
  };
  auto read_w = [&](int set, int slot) {
#pragma unroll
    for (int k2 = 0; k2 < 2; ++k2)
#pragma unroll
      for (int i = 0; i < NT; ++i)
        wf[set][k2][i] = cat8(lds_tr(lds + wa[i] + slot * WH + (16 * k2) * RB), lds_tr(lds + wa[i] + slot * WH + (16 * k2 + 4) * RB));
  };
  const unsigned gt_off = SY_TOK + (unsigned)(panel * 128 + w * 32 + r32) * 16u;
  goff[0] = hh ? gc_off[0] : gt_off; goff[1] = hh ? gc_off[2] : gc_off[1]; goff[2] = gc_off[NT - 1];
  u32x4 gt, gc[NT];
  auto load_stats = [&]() {
    gt = __builtin_amdgcn_raw_buffer_load_b128(rws, gt_off, 0, SC1);
#pragma unroll
    for (int i = 0; i < NT; ++i) gc[i] = __builtin_amdgcn_raw_buffer_load_b128(rws, gc_off[i], 0, SC1);
  };
  wait_bar<W2 + W2 / 2 + 8>();
  read_x(0, 0); read_w(0, 0);
  wait_bar<W2 + 8>();
  static_for<0, 20>([&](auto Uc) {
    constexpr int u = decltype(Uc)::value, U = u & 3;
#pragma unroll
    for (int k2 = 0; k2 < 2; ++k2) {
      const s16x8 xx = xf[U >> 1][(U & 1) * 2 + k2];
#pragma unroll
      for (int i = 0; i < NT; ++i) {
        acc[i] = MODE == 1 ? MFMA32(xx, wf[U & 1][k2][i], acc[i]) : MFMA32(wf[U & 1][k2][i], xx, acc[i]);
        SBAR();
        const int m = k2 * NT + i;
        constexpr int NG = 2 * NT - 2;
        if (u < 19) {
#pragma unroll
          for (int f = 0; f < 2 * NT; ++f)
            if (f * NG / (2 * NT) == m)
              wf[(U + 1) & 1][f / NT][f % NT] = cat8(lds_tr(lds + wa[f % NT] + ((U + 1) & 3) * WH + (16 * (f / NT)) * RB), lds_tr(lds + wa[f % NT] + ((U + 1) & 3) * WH + (16 * (f / NT) + 4) * RB));
          if (U & 1) {
#pragma unroll
            for (int r = 0; r < 4; ++r)
              if ((NT == 4 ? (r >> 1) * 3 + 1 + (r & 1) : r) == m) xf[((U + 1) >> 1) & 1][r] = lds_b128(lds + xa[r] + ((u / 2 + 1) % 3) * 16384);
          }
        }
        SBAR();
        if (m == 1) issue_w1(u + 4, 0);
        if (m == NT) issue_w1(u + 4, 1);
        if (m == 2 * NT - 2) issue_x1(u / 2 + 3, 2 * (U & 1));
        if (m == 2 * NT - 1) issue_x1(u / 2 + 3, 2 * (U & 1) + 1);
        SBAR();
      }
    }
    if (u < 18) wait_bar<(u < 14 ? W2 + 6 : u == 14 ? 8 : u == 15 ? 6 : u == 16 ? 4 : 3)>();
  });
  if (MODE == 2) __builtin_amdgcn_s_setprio(2);
  drain();
  gt = *(LAS const u32x4*)(lds + 32768 + w * 4096 + r32 * 16);
#pragma unroll
  for (int i = 0; i < NT; ++i) gc[i] = *(LAS const u32x4*)(lds + 32768 + w * 4096 + (i + 1) * 512 + r32 * 16);
  for (unsigned spin = 0; spin < SPIN_CAP; ++spin) {
    unsigned bad = (gt[0] ^ tag.a) | (gt[2] ^ tag.b);
#pragma unroll
    for (int i = 0; i < NT; ++i) bad |= (gc[i][0] ^ tag.a) | (gc[i][2] ^ tag.b);
    const bool ok = bad == 0;
    if (__all(ok)) break;
    __builtin_amdgcn_s_sleep(2);
    load_stats();
  }
  if (MODE != 2 && w == 0) flag_store(rws, SY_GO + blockIdx.x * 128u, tag);
  const float mean = __uint_as_float(gt[1]);
  float rs = __uint_as_float(gt[3]);
  if (MODE == 2) rs *= __builtin_amdgcn_rsqf(__builtin_fmaf(-EPS * rs, rs, 1.0f + EPS));
  LAS float* st_a = (LAS float*)(lds + w * 4096);
  LAS float* st_b = (LAS float*)(lds + w * 4096 + 1024);
  if (MODE == 1) {
    if (hh == 0) { st_a[r32] = rs; st_b[r32] = mean * rs; }
  } else {
#pragma unroll
    for (int i = 0; i < NT; ++i) {
      if (hh == 0) st_a[i * 32 + r32] = __uint_as_float(gc[i][1]);
      else if (MODE == 2) st_b[i * 32 + r32] = bf_lo(p.b1[n0 + i * 32 + r32]);
    }
  }
  const unsigned tile = (unsigned)(panel * 4 + w);
  if constexpr (MODE == 0) {
    const bool isq = ct < 5;
    const float sc = isq ? rs * QSCALE : rs, sm = -sc * mean;
    const unsigned hstride = isq ? 64u * 1280u : 64u * 2560u;
    const unsigned base = isq ? WS_Q + tile * 1280u : WS_KV + (tile >> 1) * 5120u;
    const unsigned f0 = (isq ? 0u : (tile & 1u) * 1024u) + (unsigned)r32 * 16u;
    const unsigned f1 = isq ? 1024u + (unsigned)r32 * 8u : 2048u + (unsigned)r32 * 16u + (tile & 1u) * 8u;
    const unsigned o16 = base + (unsigned)(H0 + ((hh + od) >> 1)) * hstride + (unsigned)((hh + od) & 1) * 512u + f0;
#pragma unroll
    for (int i = 0; i < NT; ++i) {
      unsigned pk[8];
#pragma unroll
      for (int g = 0; g < 4; ++g) {
        const f32x4 c4 = *(LAS const f32x4*)(st_a + i * 32 + 8 * g + 4 * hh);
        const f32x2 lo = (f32x2){acc[i][4 * g], acc[i][4 * g + 1]} * sc + (f32x2){c4[0], c4[1]} * sm;
        const f32x2 hi = (f32x2){acc[i][4 * g + 2], acc[i][4 * g + 3]} * sc + (f32x2){c4[2], c4[3]} * sm;
        pk[2 * g] = pack2(lo[0], lo[1]); pk[2 * g + 1] = pack2(hi[0], hi[1]);
      }
      if (i < 3) {
        __builtin_amdgcn_raw_buffer_store_b128((u32x4){pk[0], pk[1], pk[2], pk[3]}, rws, o16 + (2 * i) * hstride, 0, SC1);
        __builtin_amdgcn_raw_buffer_store_b128((u32x4){pk[4], pk[5], pk[6], pk[7]}, rws, o16 + (2 * i + 1) * hstride, 0, SC1);
      } else {
#pragma unroll
        for (int g = 0; g < 4; ++g) {
          const unsigned qd = (unsigned)(32 * c5 + quad_of(3, g, hh)), head = qd / 5u, dq = qd - head * 5u;
          const unsigned off = base + head * hstride + (dq == 4u ? f1 : f0 + (dq >> 1) * 512u + (dq & 1u) * 8u);
          __builtin_amdgcn_raw_buffer_store_b64((u32x2){pk[2 * g], pk[2 * g + 1]}, rws, off, 0, SC1);
        }
      }
      SBAR();
    }
    publish(rws, SY_FQ + ((unsigned)ct * 64u + tile) * 8u, tag);
  } else if constexpr (MODE == 1) {
    f32x4 r4[4], m4[4];
#pragma unroll
    for (int g = 0; g < 4; ++g) { r4[g] = *(LAS const f32x4*)(st_a + 8 * g + 4 * hh); m4[g] = *(LAS const f32x4*)(st_b + 8 * g + 4 * hh); }
#pragma unroll
    for (int i = 0; i < NT; ++i) {
      const float c = -__uint_as_float(gc[i][1]);
      const unsigned f = (unsigned)(c5 * 128 + i * 32 + r32), head = f / 20u, d = f - head * 20u;
      float v[16];
#pragma unroll
      for (int g = 0; g < 4; ++g)
#pragma unroll
        for (int e = 0; e < 4; ++e) v[4 * g + e] = __builtin_fmaf(acc[i][4 * g + e], r4[g][e], m4[g][e] * c);
      const unsigned off = WS_KV + head * 163840u + (tile >> 1) * 5120u + 2560u + (tile & 1u) * 1280u + d * 32u + (unsigned)hh * 16u;
#pragma unroll
      for (int s = 0; s < 2; ++s)
        __builtin_amdgcn_raw_buffer_store_b128((u32x4){pack2(v[8 * s], v[8 * s + 1]), pack2(v[8 * s + 2], v[8 * s + 3]), pack2(v[8 * s + 4], v[8 * s + 5]), pack2(v[8 * s + 6], v[8 * s + 7])}, rws, off + s * 640, 0, SC1);
      SBAR();
    }
    publish(rws, SY_FQ + ((unsigned)ct * 64u + tile) * 8u, tag);
  } else {
    const float mr = -mean * rs;
    const unsigned tok = (unsigned)(panel * 128 + w * 32 + r32);
    constexpr float G0 = -2.0f * 0.7978845608028654f * 1.4426950408889634f, G1 = G0 * 0.044715f;
    const unsigned off = WS_H + ((tok >> 6) * 192u + (unsigned)(n0 / 8 + hh)) * 1024u + (tok & 63u) * 16u;
#pragma unroll
    for (int i = 0; i < NT; ++i) {
      float v[16];
      if (i == 2) { SBAR(); __builtin_amdgcn_s_setprio(0); SBAR(); }
#pragma unroll
      for (int g = 0; g < 4; ++g) {
        const f32x4 c4 = *(LAS const f32x4*)(st_a + i * 32 + 8 * g + 4 * hh), b4 = *(LAS const f32x4*)(st_b + i * 32 + 8 * g + 4 * hh);
#pragma unroll
        for (int e = 0; e < 4; e += 2) {
          const f32x2 u = (f32x2){acc[i][4 * g + e], acc[i][4 * g + e + 1]} * rs + ((f32x2){c4[e], c4[e + 1]} * mr + (f32x2){b4[e], b4[e + 1]});
          const f32x2 z = u * ((u * u) * G1 + G0);
          const f32x2 d = (f32x2){__builtin_amdgcn_exp2f(z[0]), __builtin_amdgcn_exp2f(z[1])} + 1.0f;
          const f32x2 r = u * (f32x2){__builtin_amdgcn_rcpf(d[0]), __builtin_amdgcn_rcpf(d[1])};
          v[4 * g + e] = r[0]; v[4 * g + e + 1] = r[1];
        }
      }
#pragma unroll
      for (int s = 0; s < 2; ++s)
        __builtin_amdgcn_raw_buffer_store_b128((u32x4){pack2(v[8 * s], v[8 * s + 1]), pack2(v[8 * s + 2], v[8 * s + 3]), pack2(v[8 * s + 4], v[8 * s + 5]), pack2(v[8 * s + 6], v[8 * s + 7])}, rws, off + (i * 4 + 2 * s) * 1024, 0, SC1);
    }
    publish(rws, SY_FH + (tile * 16u + (unsigned)ct) * 8u, tag);
  }
}

DI void retile_block(const Params& p, int u, lds_ptr lds, Tag tag) {
  const int tid = threadIdx.x, lane = tid & 63, w = __builtin_amdgcn_readfirstlane(tid >> 6);
  const bool is2 = u < 96;
  const int s = (is2 ? u : u - 96) >> 1, half = u & 1;
  const rsrc_t rsrc = mk_rsrc(is2 ? p.w2 : p.wo, is2 ? F_ * D_ * 2 : D_ * D_ * 2);
  const unsigned lds0 = (unsigned)(uintptr_t)lds;
  wait_bar<0>();
#pragma unroll
  for (int i = 0; i < 5; ++i) {
    const unsigned id = (unsigned)((w * 5 + i) * 64 + lane), row = id / 40u, c = id - row * 40u;
    dma16(rsrc, row * 1280u + c * 16u, (unsigned)s * 32u * 1280u + (unsigned)half * 640u, lds0 + (unsigned)(w * 5 + i) * 1024u);
  }
  wait_bar<0>();
  const int g = lane >> 4, q = (lane & 15) >> 2, pp = lane & 3;
  const unsigned ra = (unsigned)(is2 ? 16 * (g >> 1) + 4 * (g & 1) + q : 8 * g + q) * 640u + (unsigned)w * 160u + (unsigned)pp * 8u;
  const unsigned rt = is2 ? 8u * 640u : 4u * 640u;
  const unsigned cn = (unsigned)(half * 4 + w);
  const rsrc_t rws = mk_rsrc(p.ws, WS_END);
  const unsigned dst = (is2 ? WS_W2 + (cn * 48u + (unsigned)s) * 5120u : WS_WO + (cn * 20u + (unsigned)s) * 5120u) + (unsigned)lane * 16u;
#pragma unroll
  for (int j = 0; j < 5; ++j)
    __builtin_amdgcn_raw_buffer_store_b128(__builtin_bit_cast(u32x4, cat8(lds_tr(lds + ra + j * 32), lds_tr(lds + ra + rt + j * 32))), rws, dst + j * 1024, 0, SC1);
  drain();
  flag_store(rws, SY_FW + (cn * 96u + (unsigned)(is2 ? s : 48 + s)) * 8u, tag);
  wait_bar<0>();
}

DI void colsum_block(const Params& p, int e, lds_ptr lds, Tag tag) {
  const int tid = threadIdx.x, lane = tid & 63, w = tid >> 6;
  const bool isq = e < 30;
  const unsigned ldw = isq ? 3u * D_ : (unsigned)F_;
  const unsigned short* src = (isq ? p.wqkv + e * 64 : p.w1 + (e - 30) * 64) + (size_t)(tid >> 3) * 20u * ldw + (tid & 7) * 8;
  u32x4 v[20];
#pragma unroll
  for (int r = 0; r < 20; ++r) v[r] = *(const u32x4*)(src + (size_t)r * ldw);
  float s[8] = {0.f, 0.f, 0.f, 0.f, 0.f, 0.f, 0.f, 0.f};
#pragma unroll
  for (int r = 0; r < 20; ++r)
#pragma unroll
    for (int c = 0; c < 4; ++c) { s[2 * c] += bf_lo(v[r][c]); s[2 * c + 1] += bf_hi(v[r][c]); }
#pragma unroll
  for (int c = 0; c < 8; ++c) { s[c] += __shfl_xor(s[c], 8); s[c] += __shfl_xor(s[c], 16); s[c] += __shfl_xor(s[c], 32); }
  LAS float* red = (LAS float*)lds;
  if (lane < 8) {
#pragma unroll
    for (int c = 0; c < 8; ++c) red[w * 64 + lane * 8 + c] = s[c];
  }
  __syncthreads();
  if (tid < 64) {
    const unsigned t = __float_as_uint((red[tid] + red[64 + tid]) + (red[128 + tid] + red[192 + tid]));
    __builtin_amdgcn_raw_buffer_store_b128((u32x4){tag.a, t, tag.b, t}, mk_rsrc(p.ws, WS_END), SY_COL + (unsigned)(e * 64 + tid) * 16u, 0, SC1);
  }
  wait_bar<63>();
}

DI void rowstat_block(const Params& p, int b, Tag& tag) {
  const int tid = threadIdx.x, r32 = tid & 31, row = b * 8 + (tid >> 5);
  const u32x2* src = (const u32x2*)(p.x + (size_t)row * D_) + r32;
  u32x2 v[5];
#pragma unroll
  for (int j = 0; j < 5; ++j) v[j] = src[j * 32];
  tag = load_tag(p);
  const rsrc_t rws = mk_rsrc(p.ws, WS_END);
  if (tid < 64) flag_store(rws, SY_START + blockIdx.x * 8u, tag);
  float f[20], s = 0.f;
#pragma unroll
  for (int j = 0; j < 5; ++j) { f[4 * j] = bf_lo(v[j][0]); f[4 * j + 1] = bf_hi(v[j][0]); f[4 * j + 2] = bf_lo(v[j][1]); f[4 * j + 3] = bf_hi(v[j][1]); }
#pragma unroll
  for (int j = 0; j < 20; ++j) s += f[j];
#pragma unroll
  for (int m = 1; m < 32; m <<= 1) s += __shfl_xor(s, m);
  const float mean = s * (1.0f / D_);
  float q = 0.f;
#pragma unroll
  for (int j = 0; j < 20; ++j) { const float d = f[j] - mean; q = __builtin_fmaf(d, d, q); }
#pragma unroll
  for (int m = 1; m < 32; m <<= 1) q += __shfl_xor(q, m);
  const float rs = __builtin_amdgcn_rsqf(q * (1.0f / D_) + EPS);
  if (r32 == 0) __builtin_amdgcn_raw_buffer_store_b128((u32x4){tag.a, __float_as_uint(mean), tag.b, __float_as_uint(rs)}, rws, SY_TOK + (unsigned)row * 16u, 0, SC1);
}

constexpr unsigned PAIR = 5152, SLOT = 4 * PAIR;

DI void attn_store_o(rsrc_t rws, const f32x16& o, float inv, unsigned tok, int head, int hh) {
  const unsigned base = WS_O + (tok >> 6) * 80u * 1024u + (tok & 63u) * 16u;
#pragma unroll
  for (int g = 0; g < 3; ++g) {
    const unsigned quad = (unsigned)(head * 5 + 2 * g + hh);
    const unsigned off = (g == 2 && hh) ? OOB : base + (quad >> 1) * 1024u + (quad & 1) * 8u;
    __builtin_amdgcn_raw_buffer_store_b64((u32x2){pack2(o[4 * g] * inv, o[4 * g + 1] * inv), pack2(o[4 * g + 2] * inv, o[4 * g + 3] * inv)}, rws, off, 0, SC1);
  }
}
#define PF(x) __builtin_bit_cast(s16x8, x)

DI f32x16 attn_safe(rsrc_t rws, int head, s16x8 q0, s16x8 q1, int r32, int hh) {
  f32x16 o = f32x16{};
  float m = -3.0e38f;
  const unsigned kb = WS_KV + (unsigned)head * 64u * 2560u;
  const u32x4 ones = (u32x4){0x3F803F80u, 0x3F803F80u, 0x3F803F80u, 0x3F803F80u};
  for (int t = 0; t < 64; ++t) {
    const unsigned tb = kb + (unsigned)(t >> 1) * 5120u, od = (unsigned)t & 1u;
    const u32x4 k0 = __builtin_amdgcn_raw_buffer_load_b128(rws, tb + od * 1024u + (unsigned)hh * 512u + (unsigned)r32 * 16u, 0, SC1);
    const u32x2 k1 = __builtin_amdgcn_raw_buffer_load_b64(rws, tb + 2048u + (unsigned)r32 * 16u + od * 8u, 0, SC1);
    const unsigned vo = r32 < 20 ? tb + 2560u + od * 1280u + (unsigned)r32 * 32u + (unsigned)hh * 16u : OOB;
    u32x4 v0 = __builtin_amdgcn_raw_buffer_load_b128(rws, vo, 0, SC1), v1 = __builtin_amdgcn_raw_buffer_load_b128(rws, vo, 640, SC1);
    if (r32 >= 20) { v0 = ones; v1 = ones; }
    f32x16 s = MFMA32(PF(k0), q0, f32x16{});
    s = MFMA32(PF(((u32x4){k1[0], k1[1], 0u, 0u})), q1, s);
    float mx = s[0];
#pragma unroll
    for (int r = 1; r < 16; ++r) mx = fmaxf(mx, s[r]);
    mx = fmaxf(mx, __shfl_xor(mx, 32));
    const float mn = fmaxf(m, mx), al = __builtin_amdgcn_exp2f(m - mn);
    m = mn;
#pragma unroll
    for (int r = 0; r < 16; ++r) { s[r] = __builtin_amdgcn_exp2f(s[r] - mn); o[r] *= al; }
    o = MFMA32(PF(v0), PF(((u32x4){pack2(s[0], s[1]), pack2(s[2], s[3]), pack2(s[4], s[5]), pack2(s[6], s[7])})), o);
    o = MFMA32(PF(v1), PF(((u32x4){pack2(s[8], s[9]), pack2(s[10], s[11]), pack2(s[12], s[13]), pack2(s[14], s[15])})), o);
  }
  return o;
}
DI bool attn_unsafe(const f32x16& o, float l) {
  float a = 0.f;
#pragma unroll
  for (int r = 0; r < 12; ++r) a += fabsf(o[r]);
  return !(l > 9.0e-13f && l < 1.0e12f && a < 1.0e36f);
}

DI void attn_block(const Params& p, int head, int qt, lds_ptr lds, Tag tag) {
  const int tid = threadIdx.x, lane = tid & 63, w = __builtin_amdgcn_readfirstlane(tid >> 6);
  const int r32 = lane & 31, hh = lane >> 5;
  const unsigned lds0 = (unsigned)(uintptr_t)lds;
  const rsrc_t rws = mk_rsrc(p.ws, WS_END);
  const unsigned t0 = (unsigned)(5 * head) >> 5, t1 = (unsigned)(5 * head + 4) >> 5;
  if (w == 0) {
    const unsigned row = (lane & 32 ? 10u : 5u) + (lane & 16 ? t1 : t0);
    const unsigned off[2] = {SY_FQ + (row * 64u + (((unsigned)qt * 8u + ((unsigned)lane & 15u)) & 63u)) * 8u,
                             lane < 16 ? SY_FQ + ((lane < 8 ? t0 : t1) * 64u + (unsigned)(qt * 8 + (lane & 7))) * 8u : OOB};
    poll_flags<2, 6>(rws, off, tag);
  }
  wait_bar<0>();
  const unsigned kv0 = WS_KV + (unsigned)head * 64u * 2560u + (unsigned)w * 5120u;
  {
    const unsigned qb = WS_Q + ((unsigned)head * 64u + (unsigned)(qt * 8 + w * 2)) * 1280u;
#pragma unroll
    for (int i = 0; i < 3; ++i) dma16_sc1(rws, (unsigned)lane * 16u, qb + (unsigned)i * 1024u, lds0 + 3 * SLOT + (unsigned)w * 3072u + (unsigned)i * 1024u);
  }
#pragma unroll
  for (int i = 0; i < 9; ++i)
    dma16_sc1(rws, (unsigned)lane * 16u, kv0 + (unsigned)((qt + i / 5) & 7) * 20480u + (unsigned)(i % 5) * 1024u, lds0 + (unsigned)(i / 5) * SLOT + (unsigned)w * PAIR + (unsigned)(i % 5) * 1024u);
  if (tid < 24) { const unsigned c = (tid & 1) ? 0u : 0x3F803F80u; *(LAS u32x4*)(lds + (tid >> 1) * PAIR + 5120 + (tid & 1) * 16) = (u32x4){c, c, c, c}; }
  s16x8 qf0[2], qe[2], qo[2];
  const unsigned ka0 = (unsigned)hh * 512u + (unsigned)r32 * 16u, ka1 = 2048u + (unsigned)r32 * 16u;
  unsigned va[4];
#pragma unroll
  for (int i = 0; i < 4; ++i) va[i] = r32 < 20 ? 2560u + (unsigned)i * 640u + (unsigned)r32 * 32u + (unsigned)hh * 16u : (r32 == 20 || r32 == 24) ? 5120u : 5136u;
  f32x16 oa = f32x16{}, ob = f32x16{};
  f32x16 sA0, sA1, sB0, sB1;
  s16x8 k0[2], k1[2], v0[2], v1[2];
  u32x4 pa0, pa1, pb0, pb1;
#define RD_K(slot, i) do { k0[(i) & 1] = lds_b128(lds + ka0 + (slot) + ((i) >> 1) * PAIR + ((i) & 1) * 1024); \
    if (!((i) & 1)) k1[((i) >> 1) & 1] = lds_b128(lds + ka1 + (slot) + ((i) >> 1) * PAIR); } while (0)
#define RD_V(slot, i) do { v0[(i) & 1] = lds_b128(lds + va[((i) & 1) * 2] + (slot) + ((i) >> 1) * PAIR); \
    v1[(i) & 1] = lds_b128(lds + va[((i) & 1) * 2 + 1] + (slot) + ((i) >> 1) * PAIR); } while (0)
#define EX4(S, r) do { S[r] = __builtin_amdgcn_exp2f(S[r]); S[r + 1] = __builtin_amdgcn_exp2f(S[r + 1]); S[r + 2] = __builtin_amdgcn_exp2f(S[r + 2]); S[r + 3] = __builtin_amdgcn_exp2f(S[r + 3]); \
    SBAR(); } while (0)
#define CV2(P, j, S, r) do { P[j] = pack2(S[r], S[r + 1]); P[j + 1] = pack2(S[r + 2], S[r + 3]); SBAR(); } while (0)
  const f32x16 zero = f32x16{};
  wait_bar<4>();
#pragma unroll
  for (int a = 0; a < 2; ++a) {
    qf0[a] = lds_b128(lds + 3 * SLOT + w * 3072 + a * 1280 + ka0);
    const u32x2 t = *(LAS const u32x2*)(lds + 3 * SLOT + w * 3072 + a * 1280 + 1024 + r32 * 8);
    const unsigned t0 = hh ? 0u : t[0], t1 = hh ? 0u : t[1];
    qe[a] = PF(((u32x4){t0, t1, 0u, 0u})); qo[a] = PF(((u32x4){0u, 0u, t0, t1}));
  }
  RD_K(0, 0); RD_K(0, 1); RD_V(0, 0);
  __builtin_amdgcn_s_setprio(1);
  sA0 = MFMA32(k0[0], qf0[0], zero); sA0 = MFMA32(k1[0], qe[0], sA0);
  sA1 = MFMA32(k0[0], qf0[1], zero); sA1 = MFMA32(k1[0], qe[1], sA1);
  pb1 = u32x4{}; sB1 = zero; v1[1] = v0[0];
#define Q1(T, a) ((((T) + 1) & 1) ? qo[a] : qe[a])
#define STEP(C0, C1, N0, N1, T, KS, VS, D0, D1) do { \
    __builtin_amdgcn_s_waitcnt(0xC07F); SBAR(); \
    RD_K(KS, ((T) + 2) & 7); SBAR(); \
    N0 = MFMA32(k0[((T) + 1) & 1], qf0[0], zero); SBAR();                  D0; CV2(pb1, 2, N1, 12); EX4(C0, 0); \
    ob = MFMA32(v1[((T) + 1) & 1], PF(pb1), ob); SBAR();                   RD_V(VS, ((T) + 1) & 7); SBAR(); CV2(pa0, 0, C0, 0); EX4(C0, 4); \
    N0 = MFMA32(k1[(((T) + 1) >> 1) & 1], Q1(T, 0), N0); SBAR();           CV2(pa0, 2, C0, 4); EX4(C0, 8); \
    oa = MFMA32(v0[(T) & 1], PF(pa0), oa); SBAR();                         CV2(pa1, 0, C0, 8); EX4(C0, 12); \
    N1 = MFMA32(k0[((T) + 1) & 1], qf0[1], zero); SBAR();                  D1; CV2(pa1, 2, C0, 12); EX4(C1, 0); \
    oa = MFMA32(v1[(T) & 1], PF(pa1), oa); SBAR();                         CV2(pb0, 0, C1, 0); EX4(C1, 4); \
    N1 = MFMA32(k1[(((T) + 1) >> 1) & 1], Q1(T, 1), N1); SBAR();           CV2(pb0, 2, C1, 4); EX4(C1, 8); \
    ob = MFMA32(v0[(T) & 1], PF(pb0), ob); SBAR();                         CV2(pb1, 0, C1, 8); EX4(C1, 12); \
  } while (0)
#define KVP(cc, sl, i) do { dma16_sc1(mk_rsrc(p.ws, (cc) < 8 ? WS_END : 0), (unsigned)lane * 16u, kv0 + (unsigned)((qt + (cc)) & 7) * 20480u + (i) * 1024u, lds0 + (sl) + (unsigned)w * PAIR + (i) * 1024u); SBAR(); } while (0)
#define NOP_ do { } while (0)
#define HINT do { __builtin_amdgcn_raw_buffer_store_b64((u32x2){tag.a, tag.b}, rws, (c == MLP2_GATE && tid == 0) ? SY_GO + 64u + blockIdx.x * 128u : OOB, 0, SC1); SBAR(); } while (0)
#define FLG do { dma16_half_sc1(mk_rsrc(p.ws, c == 0 ? WS_END : 0), (unsigned)lane * 16u, SY_FQ + ((w & 2 ? 10u : 5u) + (w & 1 ? t1 : t0)) * 512u, lds0 + 3 * SLOT + 12288u + (unsigned)w * 512u); SBAR(); } while (0)
  unsigned cur = 0, nxt = SLOT, prv = 2 * SLOT;
  for (int c = 0; c < 8; ++c) {
    STEP(sA0, sA1, sB0, sB1, 0, cur, cur, KVP(c + 1, nxt, 4), NOP_);
    STEP(sB0, sB1, sA0, sA1, 1, cur, cur, HINT, NOP_);
    STEP(sA0, sA1, sB0, sB1, 2, cur, cur, NOP_, NOP_);
    STEP(sB0, sB1, sA0, sA1, 3, cur, cur, NOP_, NOP_);
    STEP(sA0, sA1, sB0, sB1, 4, cur, cur, FLG, NOP_);
    STEP(sB0, sB1, sA0, sA1, 5, cur, cur, NOP_, NOP_);
    wait_bar<0>();
    if (c == 0) {
      const u32x4 fa = *(LAS const u32x4*)(lds + 3 * SLOT + 12288 + lane * 16), fb = *(LAS const u32x4*)(lds + 3 * SLOT + 13312 + lane * 16);
      const unsigned bad = (fa[0] ^ tag.a) | (fa[1] ^ tag.b) | (fa[2] ^ tag.a) | (fa[3] ^ tag.b) | (fb[0] ^ tag.a) | (fb[1] ^ tag.b) | (fb[2] ^ tag.a) | (fb[3] ^ tag.b);
      if (__builtin_expect(__any(bad != 0u), 0)) {
        const unsigned l8 = (unsigned)lane * 8u;
        const unsigned off[4] = {SY_FQ + (5 + t0) * 512u + l8, SY_FQ + (5 + t1) * 512u + l8, SY_FQ + (10 + t0) * 512u + l8, SY_FQ + (10 + t1) * 512u + l8};
        poll_flags<4>(rws, off, tag);
      }
    }
    STEP(sA0, sA1, sB0, sB1, 6, nxt, cur, KVP(c + 2, prv, 0), KVP(c + 2, prv, 1));
    STEP(sB0, sB1, sA0, sA1, 7, nxt, nxt, KVP(c + 2, prv, 2), KVP(c + 2, prv, 3));
    const unsigned t_ = cur; cur = nxt; nxt = prv; prv = t_;
  }
  __builtin_amdgcn_s_setprio(3);
  CV2(pb1, 2, sB1, 12);
  ob = MFMA32(v1[1], PF(pb1), ob);
  float la = hh ? oa[8] : oa[12], lb = hh ? ob[8] : ob[12];
  if (__builtin_expect(__any(attn_unsafe(oa, la) || attn_unsafe(ob, lb)), 0)) {
    oa = attn_safe(rws, head, qf0[0], qe[0], r32, hh);
    ob = attn_safe(rws, head, qf0[1], qe[1], r32, hh);
    la = hh ? oa[8] : oa[12]; lb = hh ? ob[8] : ob[12];
  }
  const unsigned tok = (unsigned)(qt * 256 + w * 64 + r32);
  attn_store_o(rws, oa, __builtin_amdgcn_rcpf(la), tok, head, hh);
  attn_store_o(rws, ob, __builtin_amdgcn_rcpf(lb), tok + 32, head, hh);
  publish(rws, SY_FO + ((unsigned)(qt * 4 + w) * 32u + (unsigned)head) * 8u, tag);
}

constexpr unsigned ST2 = 18432;
DI void tail_block(const Params& p, int rt, int cn, lds_ptr lds, Tag tag) {
  const int tid = threadIdx.x, lane = tid & 63, w = __builtin_amdgcn_readfirstlane(tid >> 6);
  const unsigned lds0 = (unsigned)(uintptr_t)lds;
  const rsrc_t rws = mk_rsrc(p.ws, WS_END);
  const unsigned n0 = (unsigned)cn * 80u, l16 = (unsigned)lane * 16u;
  const int g = lane >> 4;
  const unsigned afrag = (unsigned)g * 1024u + (unsigned)(w * 16 + (lane & 15)) * 16u;
  const unsigned tok = (unsigned)(rt * 64 + w * 16 + (lane & 15));
  u32x2 xb[5], bb[5];
#pragma unroll
  for (int j = 0; j < 5; ++j) { xb[j] = *(const u32x2*)(p.x + tok * D_ + n0 + j * 16 + g * 4); bb[j] = *(const u32x2*)(p.b2 + n0 + j * 16 + g * 4); }
  if (w == 0) {
    const unsigned off[2] = {lane < 32 ? SY_FH + ((unsigned)rt * 32u + (unsigned)lane) * 8u : lane < 36 ? SY_FW + ((unsigned)cn * 96u + 32u + (unsigned)lane) * 8u : OOB,
                             SY_FW + ((unsigned)cn * 96u + (unsigned)lane) * 8u};
    poll_flags<2, 6>(rws, off, tag);
  }
  wait_bar<0>();
  __builtin_amdgcn_s_setprio(0);
  const unsigned wo0 = WS_WO + (unsigned)cn * 102400u;
  u32x4 wr[4][5];
#pragma unroll
  for (int s = 0; s < 4; ++s)
#pragma unroll
    for (int j = 0; j < 5; ++j) wr[s][j] = __builtin_amdgcn_raw_buffer_load_b128(rws, l16, wo0 + (unsigned)((16 + s) * 5 + j) * 1024u, SC1);
#pragma unroll
  for (int s = 0; s < 4; ++s)
#pragma unroll
    for (int j = 0; j < 5; ++j) asm volatile("" : "+v"(wr[s][j]));
#pragma unroll
  for (int j = 0; j < 5; ++j) asm volatile("" : "+v"(xb[j]), "+v"(bb[j]));
  f32x4 am[5], as[5];
#pragma unroll
  for (int j = 0; j < 5; ++j) { am[j] = f32x4{}; as[j] = f32x4{}; }
  {
    const unsigned a0 = WS_H + (unsigned)rt * 192u * 1024u, w0 = WS_W2 + (unsigned)cn * 245760u;
    auto issue1 = [&](int st, unsigned slot, bool wo, int i) {
      const unsigned d = i < 2 ? (unsigned)w * 2048u + (unsigned)i * 1024u : i < 4 ? 8192u + (unsigned)(i - 2) * 4096u + (unsigned)w * 1024u : 16384u + (unsigned)w * 512u;
      const unsigned src = wo ? wo0 + slot + d : i < 2 ? a0 + (unsigned)st * 8192u + (unsigned)i * 4096u : w0 + (unsigned)st * 10240u + d - 8192u;
      if (i == 4) dma16_half_sc1(rws, l16, src, lds0 + slot + d);
      else dma16_sc1(rws, (i < 2 && !wo) ? afrag : l16, src, lds0 + slot + d);
    };
    auto issue = [&](int st, unsigned slot, bool wo) {
#pragma unroll
      for (int i = 0; i < 5; ++i) issue1(st, slot, wo, i);
    };
    dma16_sc1(rws, l16, wo0 + 4 * ST2 + (unsigned)w * 2048u, lds0 + 4 * ST2 + (unsigned)w * 2048u);
    dma16_sc1(rws, l16, wo0 + 4 * ST2 + (unsigned)w * 2048u + 1024u, lds0 + 4 * ST2 + (unsigned)w * 2048u + 1024u);
    issue(0, 0, false); issue(1, ST2, false); issue(2, 2 * ST2, false); issue(3, 3 * ST2, false);
    const unsigned aa = (unsigned)w * 2048u + l16;
    s16x8 af[2][2], wf[2][10];
    if (w == 0) {
      const unsigned off[1] = {lane == 0 ? SY_GO + 64u + (blockIdx.x & 255u) * 128u : OOB};
      poll_flags<1, 16>(rws, off, tag);
    }
    wait_bar<15>();
#pragma unroll
    for (int s = 0; s < 2; ++s) af[0][s] = lds_b128(lds + aa + s * 1024);
#pragma unroll
    for (int f = 0; f < 10; ++f) wf[0][f] = lds_b128(lds + l16 + 8192 + f * 1024);
    wait_bar<10>();
    auto stage = [&](int st, auto Uc, bool wo) {
      constexpr int U = decltype(Uc)::value;
      constexpr unsigned nx = ((U + 1) & 3) * ST2;
#pragma unroll
      for (int m = 0; m < 10; ++m) {
        am[m % 5] = MFMA16(wf[U & 1][m], af[U & 1][m / 5], am[m % 5]);
        SBAR();
        if (m == 0) { af[(U + 1) & 1][0] = lds_b128(lds + aa + nx); af[(U + 1) & 1][1] = lds_b128(lds + aa + nx + 1024); }
        if (m >= 1 && m < 6) { wf[(U + 1) & 1][2 * m - 2] = lds_b128(lds + l16 + nx + 8192 + (2 * m - 2) * 1024); wf[(U + 1) & 1][2 * m - 1] = lds_b128(lds + l16 + nx + 8192 + (2 * m - 1) * 1024); }
        SBAR();
        if (m & 1) issue1(st + 4, U * ST2, wo, m >> 1);
        SBAR();
      }
      wait_bar<10>();
    };
    for (int st = 0; st < 20; st += 4) {
      stage(st, std::integral_constant<int, 0>{}, false); stage(st + 1, std::integral_constant<int, 1>{}, false);
      stage(st + 2, std::integral_constant<int, 2>{}, false); stage(st + 3, std::integral_constant<int, 3>{}, false);
    }
    stage(20, std::integral_constant<int, 0>{}, true); stage(21, std::integral_constant<int, 1>{}, true);
    stage(22, std::integral_constant<int, 2>{}, true); stage(23, std::integral_constant<int, 3>{}, true);
  }
  __builtin_amdgcn_s_setprio(2);
#pragma unroll
  for (int j = 0; j < 5; ++j) {
#pragma unroll
    for (int e = 0; e < 4; ++e) {
      const float m = am[j][e] + (e & 1 ? bf_hi(bb[j][e >> 1]) : bf_lo(bb[j][e >> 1]));
      am[j][e] = sigmoid_mul(m, m * -1.4426950408889634f);
    }
    asm volatile("" : "+v"(am[j]));
  }
  wait_bar<0>();
  {
    const unsigned fo = lane < 32 ? SY_FO + ((unsigned)rt * 32u + (unsigned)lane) * 8u : OOB;
    f32x4 a2[5];
#pragma unroll
    for (int j = 0; j < 5; ++j) a2[j] = f32x4{};
    u32x4 of[20];
    auto request = [&](auto Gc) {
      constexpr int G = decltype(Gc)::value;
#pragma unroll
      for (int s = 5 * G; s < 5 * G + 5; ++s) of[s] = __builtin_amdgcn_raw_buffer_load_b128(rws, afrag, WS_O + (unsigned)rt * 81920u + (unsigned)s * 4096u, SC1);
    };
    auto compute = [&](auto Gc) {
      constexpr int G = decltype(Gc)::value;
#pragma unroll
      for (int s = 5 * G; s < 5 * G + 5; ++s)
#pragma unroll
        for (int j = 0; j < 5; ++j) {
          const s16x8 wf = s < 16 ? lds_b128(lds + l16 + (s * 5 + j) * 1024) : PF(wr[s < 16 ? 0 : s - 16][j]);
          if (G < 2) as[j] = MFMA16(wf, PF(of[s]), as[j]); else a2[j] = MFMA16(wf, PF(of[s]), a2[j]);
        }
    };
    auto missing_now = [&]() -> unsigned {
      const u32x2 v = __builtin_amdgcn_raw_buffer_load_b64(rws, fo, 0, SC1);
      const unsigned m = (unsigned)__ballot(lane < 32 && (((v[0] ^ tag.a) | (v[1] ^ tag.b)) != 0u));
      asm volatile("" ::: "memory");
      return m;
    };
    using I0 = std::integral_constant<int, 0>; using I1 = std::integral_constant<int, 1>;
    using I2 = std::integral_constant<int, 2>; using I3 = std::integral_constant<int, 3>;
    unsigned missing = missing_now();
    if (missing == 0u) {
      request(I0{}); request(I2{}); request(I1{}); request(I3{});
      SBAR();
      compute(I0{}); compute(I2{}); compute(I1{}); compute(I3{});
    } else {
#pragma unroll
      for (int s = 0; s < 20; ++s) of[s] = u32x4{};
      unsigned req = 0u, done = 0u;
      for (unsigned spin = 0;; ++spin) {
        const unsigned r0 = req;
        if (!(req & 1u) && !(missing & 0x000000FFu)) { request(I0{}); req |= 1u; }
        if (!(req & 2u) && !(missing & 0x0000FF00u)) { request(I1{}); req |= 2u; }
        if (!(req & 4u) && !(missing & 0x00FF0000u)) { request(I2{}); req |= 4u; }
        if (!(req & 8u) && !(missing & 0xFF000000u)) { request(I3{}); req |= 8u; }
        if ((req & ~done & 1u)) { compute(I0{}); done |= 1u; }
        if ((req & ~done & 4u)) { compute(I2{}); done |= 4u; }
        if ((req & ~done & 2u) && (done & 1u)) { compute(I1{}); done |= 2u; }
        if ((req & ~done & 8u) && (done & 4u)) { compute(I3{}); done |= 8u; }
        if (done == 15u) break;
        if (req == r0) { if (__builtin_popcount(missing) > 6) __builtin_amdgcn_s_sleep(24); else __builtin_amdgcn_s_sleep(3); }
        missing = spin < SPIN_CAP ? missing_now() : 0u;
      }
    }
#pragma unroll
    for (int j = 0; j < 5; ++j) as[j] += a2[j];
  }
#pragma unroll
  for (int j = 0; j < 5; ++j) {
    float r[4];
#pragma unroll
    for (int e = 0; e < 4; ++e) r[e] = __builtin_fmaf(as[j][e], am[j][e], e & 1 ? bf_hi(xb[j][e >> 1]) : bf_lo(xb[j][e >> 1]));
    __builtin_amdgcn_raw_buffer_store_b64((u32x2){pack2(r[0], r[1]), pack2(r[2], r[3])}, mk_rsrc(p.out, S_ * D_ * 2), (tok * D_ + n0 + j * 16 + g * 4) * 2u, 0, 0);
  }
}

__global__ __launch_bounds__(256, 2) void fused_block(Params p) {
  extern __shared__ __attribute__((aligned(16))) char smem[];
  lds_ptr lds = (lds_ptr)smem;
  const int b = blockIdx.x & 255, xcd = b & 7, j = b >> 3;
  const int xq = (xcd + 5) & 7;
  const int panel = (xq & 1) * 8 + (j & 7), c = (xq >> 1) * 4 + (j >> 3);
  Tag tag;
  if (blockIdx.x < 256) {
    __builtin_amdgcn_s_setprio(3);
    if (c < 10) gemm_xw_tile<0>(p, panel, c, lds, tag);
    else if (c < 15) gemm_xw_tile<1>(p, panel, c, lds, tag);
    else {
      const rsrc_t rws = mk_rsrc(p.ws, WS_END);
      tag = load_tag(p);
      if (threadIdx.x < 64) flag_store(rws, SY_START + blockIdx.x * 8u, tag);
      colsum_block(p, panel, lds, tag);
      colsum_block(p, 30 + panel, lds, tag);
      if (panel == 15 && threadIdx.x < 64) {
        unsigned off[8];
#pragma unroll
        for (int i = 0; i < 8; ++i) off[i] = SY_START + (unsigned)(i * 64 + (int)threadIdx.x) * 8u;
        poll_flags<8, 16>(rws, off, tag);
        flag_store(rws, SY_GEN, tag);
      }
    }
    attn_block(p, xcd * 4 + (j >> 3), j & 7, lds, tag);
    asm volatile(".Lpf_end:");
  } else {
    __builtin_amdgcn_s_setprio(2);
    unsigned cpf = 0;
    {
      unsigned long long pc; int len;
      asm volatile("s_getpc_b64 %0\n.Lpf_here:\n\ts_mov_b32 %1, .Lpf_end-.Lpf_here" : "=s"(pc), "=s"(len));
      const unsigned o = (threadIdx.x * 32u + (unsigned)j) * 128u;
      if (threadIdx.x < 64 && (int)o < len) cpf = *(const volatile unsigned*)(pc + o);
    }
    rowstat_block(p, b, tag);
    if (c == 15 && panel < 14) colsum_block(p, 16 + panel, lds, tag);
    if (c == 15 && panel >= 8) colsum_block(p, 38 + panel, lds, tag);
    if (b < 136) retile_block(p, b, lds, tag);
    asm volatile("" ::"v"(cpf));
    __builtin_amdgcn_s_setprio(2);
    gemm_xw_tile<2>(p, panel, c, lds, tag);
    tail_block(p, (xcd >> 1) * 8 + (j & 7), (xcd & 1) * 4 + (j >> 3), lds, tag);
  }
}

extern "C" void kernel_launch(void* const* d_in, const int* in_sizes, int n_in, void* d_out, int out_size, void* d_ws, size_t ws_size, hipStream_t stream) {
  Params p;
  p.x = (const unsigned short*)d_in[0];
  p.wqkv = (const unsigned short*)d_in[1];
  p.wo = (const unsigned short*)d_in[2];
  p.w1 = (const unsigned short*)d_in[4];
  p.b1 = (const unsigned short*)d_in[5];
  p.w2 = (const unsigned short*)d_in[6];
  p.b2 = (const unsigned short*)d_in[7];
  p.out = (unsigned short*)d_out;
  p.ws = (char*)d_ws;
  (void)hipFuncSetAttribute((const void*)fused_block, hipFuncAttributeMaxDynamicSharedMemorySize, LDS_BYTES);
  fused_block<<<512, 256, LDS_BYTES, stream>>>(p);
}
```

```cpp
#include <hip/hip_runtime.h>
#include <stdint.h>
#include <type_traits>

typedef __attribute__((ext_vector_type(8))) short s16x8;
typedef __attribute__((ext_vector_type(4))) short s16x4;
typedef __attribute__((ext_vector_type(16))) float f32x16;
typedef __attribute__((ext_vector_type(4))) float f32x4;
typedef __attribute__((ext_vector_type(2))) float f32x2;
typedef __attribute__((ext_vector_type(4))) unsigned u32x4;
typedef __attribute__((ext_vector_type(2))) unsigned u32x2;
typedef __attribute__((ext_vector_type(2))) __bf16 bf16x2;
#define LAS __attribute__((address_space(3)))
#define DI __device__ __forceinline__
typedef LAS char* lds_ptr;
typedef __amdgpu_buffer_rsrc_t rsrc_t;
template <int I, int N, typename F> DI void static_for(F&& f) {
  if constexpr (I < N) { f(std::integral_constant<int, I>{}); static_for<I + 1, N>(f); }
}

constexpr int S_ = 2048, D_ = 640, F_ = 1536;
constexpr float EPS = 1e-5f;
constexpr float QSCALE = 0.22360679774997896f * 1.4426950408889634f;
constexpr int SC1 = 16;
constexpr unsigned OOB = 0x80000000u;

constexpr unsigned WS_Q = 0;
constexpr unsigned WS_KV = WS_Q + 32u * 64u * 1280u;
constexpr unsigned WS_H = WS_KV + 32u * 64u * 2560u;
constexpr unsigned WS_O = WS_H + 2048u * 1536u * 2u;
constexpr unsigned WS_W2 = WS_O + 2048u * 640u * 2u;
constexpr unsigned WS_WO = WS_W2 + 1536u * 640u * 2u;
constexpr unsigned SY_GEN = WS_WO + 640u * 640u * 2u;
constexpr unsigned SY_START = SY_GEN + 256;
constexpr unsigned SY_GO = SY_START + 512 * 8;
constexpr unsigned SY_FQ = SY_GO + 256 * 128;
constexpr unsigned SY_FH = SY_FQ + 16 * 64 * 8;
constexpr unsigned SY_FO = SY_FH + 64 * 16 * 8;
constexpr unsigned SY_FW = SY_FO + 32 * 32 * 8;
constexpr unsigned SY_TOK = SY_FW + 8 * 96 * 8;
constexpr unsigned SY_COL = SY_TOK + 2048 * 16;
constexpr unsigned WS_END = SY_COL + 3456 * 16;

constexpr int LDS_BYTES = 81920;
constexpr unsigned SPIN_CAP = 1u << 20;
constexpr int MLP2_GATE = 6;

DI unsigned pack2(float a, float b) { return __builtin_bit_cast(unsigned, __builtin_convertvector((f32x2){a, b}, bf16x2)); }
DI float bf_lo(unsigned v) { return __uint_as_float(v << 16); }
DI float bf_hi(unsigned v) { return __uint_as_float(v & 0xFFFF0000u); }
DI rsrc_t mk_rsrc(const void* p, unsigned bytes) { return __builtin_amdgcn_make_buffer_rsrc((void*)p, (short)0, (int)bytes, 0x00020000); }
DI void dma16(rsrc_t rs, unsigned voff, unsigned soff, unsigned ldsaddr) {
  asm volatile("s_mov_b32 m0, %0\n\ts_nop 0\n\tbuffer_load_dwordx4 %1, %2, %3 offen lds" ::"s"(ldsaddr), "v"(voff), "s"(rs), "s"(soff) : "memory");
}
DI void dma16_sc1(rsrc_t rs, unsigned voff, unsigned soff, unsigned ldsaddr) {
  asm volatile("s_mov_b32 m0, %0\n\ts_nop 0\n\tbuffer_load_dwordx4 %1, %2, %3 offen sc1 lds" ::"s"(ldsaddr), "v"(voff), "s"(rs), "s"(soff) : "memory");
}
DI void dma16_half(rsrc_t rs, unsigned voff, unsigned soff, unsigned ldsaddr) {
  asm volatile("s_mov_b32 m0, %0\n\ts_mov_b64 exec, 0xffffffff\n\tbuffer_load_dwordx4 %1, %2, %3 offen lds\n\ts_mov_b64 exec, -1" ::"s"(ldsaddr), "v"(voff), "s"(rs), "s"(soff) : "memory");
}
DI void dma16_half_sc1(rsrc_t rs, unsigned voff, unsigned soff, unsigned ldsaddr) {
  asm volatile("s_mov_b32 m0, %0\n\ts_mov_b64 exec, 0xffffffff\n\tbuffer_load_dwordx4 %1, %2, %3 offen sc1 lds\n\ts_mov_b64 exec, -1" ::"s"(ldsaddr), "v"(voff), "s"(rs), "s"(soff) : "memory");
}
template <int N> DI void wait_bar() { asm volatile("s_waitcnt vmcnt(%0) lgkmcnt(0)\n\ts_barrier" ::"i"(N) : "memory"); }
DI void drain() { asm volatile("s_waitcnt vmcnt(0)" ::: "memory"); }
#define SBAR() __builtin_amdgcn_sched_barrier(0)
#define MFMA32(a, b, c) __builtin_amdgcn_mfma_f32_32x32x16_bf16((a), (b), (c), 0, 0, 0)
#define MFMA16(a, b, c) __builtin_amdgcn_mfma_f32_16x16x32_bf16((a), (b), (c), 0, 0, 0)
DI s16x8 lds_b128(lds_ptr p) { return *(LAS const s16x8*)p; }
DI s16x4 lds_tr(lds_ptr p) { return __builtin_amdgcn_ds_read_tr16_b64_v4i16((LAS s16x4*)p); }
DI s16x8 cat8(s16x4 a, s16x4 b) { return __builtin_shufflevector(a, b, 0, 1, 2, 3, 4, 5, 6, 7); }
DI float sigmoid_mul(float u, float w  ) { return u * __builtin_amdgcn_rcpf(1.0f + __builtin_amdgcn_exp2f(w)); }

struct Params {
  const unsigned short* x;
  const unsigned short* wqkv;
  const unsigned short* wo;
  const unsigned short* w1;
  const unsigned short* b1;
  const unsigned short* w2;
  const unsigned short* b2;
  unsigned short* out;
  char* ws;
};
struct Tag { unsigned a, b; };

DI Tag load_tag(const Params& p) {
  unsigned long long g;
  asm volatile("s_load_dwordx2 %0, %1, 0x0\n\ts_waitcnt lgkmcnt(0)" : "=s"(g) : "s"(p.ws + SY_GEN) : "memory");
  g += 0x9E3779B97F4A7C15ull; g = (g ^ (g >> 30)) * 0xBF58476D1CE4E5B9ull; g = (g ^ (g >> 27)) * 0x94D049BB133111EBull; g ^= g >> 31;
  return Tag{(unsigned)g, (unsigned)(g >> 32)};
}
DI void flag_store(rsrc_t rws, unsigned off, Tag t) {
  if ((threadIdx.x & 63) == 0) __builtin_amdgcn_raw_buffer_store_b64((u32x2){t.a, t.b}, rws, off, 0, SC1);
}
DI void publish(rsrc_t rws, unsigned off, Tag t) { drain(); flag_store(rws, off, t); }
template <int N, int SLEEP = 2> DI void poll_flags(rsrc_t rws, const unsigned (&off)[N], Tag t) {
  for (unsigned spin = 0; spin < SPIN_CAP; ++spin) {
    u32x2 v[N];
#pragma unroll
    for (int i = 0; i < N; ++i) v[i] = __builtin_amdgcn_raw_buffer_load_b64(rws, off[i], 0, SC1);
    unsigned bad = 0;
#pragma unroll
    for (int i = 0; i < N; ++i) bad |= off[i] == OOB ? 0u : ((v[i][0] ^ t.a) | (v[i][1] ^ t.b));
    const bool ok = bad == 0;
    if (__all(ok)) break;
    __builtin_amdgcn_s_sleep(SLEEP);
  }
  asm volatile("" ::: "memory");
}

template <int MODE>
DI void gemm_xw_tile(const Params& p, int panel, int ct, lds_ptr lds, Tag& tag) {
  constexpr int NT = MODE == 2 ? 3 : 4;
  constexpr unsigned LDW = MODE == 2 ? F_ * 2u : 3u * D_ * 2u;
  constexpr unsigned RB = NT * 64, WH = 32 * RB, WSL = 49152;
  constexpr int W2 = 4;
  const int tid = threadIdx.x, lane = tid & 63, w = __builtin_amdgcn_readfirstlane(tid >> 6);
  const int r32 = lane & 31, hh = lane >> 5;
  const int n0 = ct * NT * 32;
  const unsigned lds0 = (unsigned)(uintptr_t)lds;
  unsigned xv[2], wv[2];
#pragma unroll
  for (int i = 0; i < 2; ++i) {
    const int row = 8 * i + (lane >> 3);
    xv[i] = (unsigned)row * 1280u + (unsigned)(((lane & 7) ^ ((row >> 1) & 7)) * 16);
  }
  const unsigned xs0 = (unsigned)(panel * 128 + w * 32) * 1280u;
  const unsigned wd1 = NT == 4 ? 4096u + (unsigned)w * 1024u : 4096u + (unsigned)w * 512u;
#pragma unroll
  for (int j = 0; j < 2; ++j) {
    const unsigned id = (j == 0 ? (unsigned)w * 1024u : wd1) / 16u + (unsigned)lane;
    if (NT == 4) {
      const unsigned row = id >> 4, c = (id & 15u) ^ ((row & 3u) << 2);
      wv[j] = row * LDW + c * 16u + (unsigned)n0 * 2u;
    } else {
      const unsigned row = id / 12u, c = id - row * 12u;
      wv[j] = row * LDW + c * 16u + (unsigned)n0 * 2u;
    }
  }
  const rsrc_t rws = mk_rsrc(p.ws, WS_END);
  unsigned goff[3];
  auto issue_x1 = [&](int st, int i) {
    const unsigned dst = lds0 + (unsigned)(st % 3) * 16384u + (unsigned)w * 4096u + (unsigned)i * 1024u;
    if (st < 10) dma16(mk_rsrc(p.x, S_ * D_ * 2), xv[i & 1], xs0 + (unsigned)st * 128u + (unsigned)(i >> 1) * 20480u, dst);
    else if (st == 11 && i < 3) dma16_sc1(rws, goff[i < 3 ? i : 0], 0u, dst);
  };
  auto issue_w1 = [&](int u, int j) {
    if (u >= 20) return;
    const rsrc_t rw = mk_rsrc(MODE == 2 ? p.w1 : p.wqkv, D_ * LDW);
    const unsigned dst = lds0 + WSL + (unsigned)(u & 3) * WH + (j == 0 ? (unsigned)w * 1024u : wd1);
    if (NT == 3 && j == 1) dma16_half(rw, wv[j], (unsigned)u * 32u * LDW, dst);
    else dma16(rw, wv[j], (unsigned)u * 32u * LDW, dst);
  };
  auto issue_x = [&](int st) {
#pragma unroll
    for (int i = 0; i < 4; ++i) issue_x1(st, i);
  };
  auto issue_w = [&](int u) { issue_w1(u, 0); issue_w1(u, 1); };
  if (MODE == 2 && ct < 15) {
    for (int i = 0; i < 4; ++i) __builtin_amdgcn_s_sleep(16);
  }
  issue_x(0); issue_w(0); issue_w(1); issue_x(1); issue_w(2); issue_w(3); issue_x(2);
  if (MODE != 2) {
    tag = load_tag(p);
    if (w == 0) flag_store(rws, SY_START + blockIdx.x * 8u, tag);
  } else if (ct < 15 && w == 0) {
    const unsigned off[1] = {lane == 0 ? SY_GO + (blockIdx.x & 255u) * 128u : OOB};
    poll_flags<1, 8>(rws, off, tag);
  }
  const int c5 = ct % 5, od = c5 & 1, H0 = (32 * c5 + (od ? 0 : 4)) / 5;
  const unsigned long long singles = c5 == 0 ? 0x1F1E1D18130E0904ull : c5 == 1 ? 0x1F1E1B16110C0702ull : c5 == 2 ? 0x1F1E19140F0A0500ull
                                   : c5 == 3 ? 0x1F1C17120D080300ull : 0x1F1A15100B060100ull;
  auto quad_of = [&](int i, int g, int h) -> int {
    if (MODE != 0) return 8 * i + 2 * g + h;
    if (i == 3) return (int)((singles >> (8 * (2 * g + h))) & 31ull);
    const int kk = (2 * i + (g >> 1)) * 2 + h + od;
    return 5 * (H0 + (kk >> 1)) + 2 * (kk & 1) + (g & 1) - 32 * c5;
  };
  unsigned xa[4], wa[NT], gc_off[NT];
  {
    const int row = w * 32 + r32, z = (row >> 1) & 7;
#pragma unroll
    for (int ks = 0; ks < 4; ++ks) xa[ks] = (unsigned)row * 128u + (unsigned)(((2 * ks + hh) ^ z) * 16);
    const int gq = lane >> 4, q = (lane & 15) >> 2, pp = lane & 3;
#pragma unroll
    for (int i = 0; i < NT; ++i) {
      const unsigned col = (unsigned)quad_of(i, 2 * (gq & 1) + (pp >> 1), pp & 1) * 8u;
      wa[i] = WSL + (unsigned)(8 * (gq >> 1) + q) * RB + (NT == 4 ? col ^ ((unsigned)q << 6) : col);
      gc_off[i] = SY_COL + (unsigned)((MODE == 2 ? 1920 : 0) + n0 + 4 * quad_of(i, r32 >> 3, (r32 >> 2) & 1) + (r32 & 3)) * 16u;
    }
  }
  f32x16 acc[NT];
#pragma unroll
  for (int i = 0; i < NT; ++i) acc[i] = f32x16{};
  s16x8 xf[2][4], wf[2][2][NT];
  auto read_x = [&](int set, unsigned slot) {
#pragma unroll
    for (int ks = 0; ks < 4; ++ks) xf[set][ks] = lds_b128(lds + xa[ks] + slot);
  };
  auto read_w = [&](int set, int slot) {
#pragma unroll
    for (int k2 = 0; k2 < 2; ++k2)
#pragma unroll
      for (int i = 0; i < NT; ++i)
        wf[set][k2][i] = cat8(lds_tr(lds + wa[i] + slot * WH + (16 * k2) * RB), lds_tr(lds + wa[i] + slot * WH + (16 * k2 + 4) * RB));
  };
  const unsigned gt_off = SY_TOK + (unsigned)(panel * 128 + w * 32 + r32) * 16u;
  goff[0] = hh ? gc_off[0] : gt_off; goff[1] = hh ? gc_off[2] : gc_off[1]; goff[2] = gc_off[NT - 1];
  u32x4 gt, gc[NT];
  auto load_stats = [&]() {
    gt = __builtin_amdgcn_raw_buffer_load_b128(rws, gt_off, 0, SC1);
#pragma unroll
    for (int i = 0; i < NT; ++i) gc[i] = __builtin_amdgcn_raw_buffer_load_b128(rws, gc_off[i], 0, SC1);
  };
  wait_bar<W2 + W2 / 2 + 8>();
  read_x(0, 0); read_w(0, 0);
  wait_bar<W2 + 8>();
  static_for<0, 20>([&](auto Uc) {
    constexpr int u = decltype(Uc)::value, U = u & 3;
#pragma unroll
    for (int k2 = 0; k2 < 2; ++k2) {
      const s16x8 xx = xf[U >> 1][(U & 1) * 2 + k2];
#pragma unroll
      for (int i = 0; i < NT; ++i) {
        acc[i] = MODE == 1 ? MFMA32(xx, wf[U & 1][k2][i], acc[i]) : MFMA32(wf[U & 1][k2][i], xx, acc[i]);
        SBAR();
        const int m = k2 * NT + i;
        constexpr int NG = 2 * NT - 2;
        if (u < 19) {
#pragma unroll
          for (int f = 0; f < 2 * NT; ++f)
            if (f * NG / (2 * NT) == m)
              wf[(U + 1) & 1][f / NT][f % NT] = cat8(lds_tr(lds + wa[f % NT] + ((U + 1) & 3) * WH + (16 * (f / NT)) * RB), lds_tr(lds + wa[f % NT] + ((U + 1) & 3) * WH + (16 * (f / NT) + 4) * RB));
          if (U & 1) {
#pragma unroll
            for (int r = 0; r < 4; ++r)
              if ((NT == 4 ? (r >> 1) * 3 + 1 + (r & 1) : r) == m) xf[((U + 1) >> 1) & 1][r] = lds_b128(lds + xa[r] + ((u / 2 + 1) % 3) * 16384);
          }
        }
        SBAR();
        if (m == 1) issue_w1(u + 4, 0);
        if (m == NT) issue_w1(u + 4, 1);
        if (m == 2 * NT - 2) issue_x1(u / 2 + 3, 2 * (U & 1));
        if (m == 2 * NT - 1) issue_x1(u / 2 + 3, 2 * (U & 1) + 1);
        SBAR();
      }
    }
    if (u < 18) wait_bar<(u < 14 ? W2 + 6 : u == 14 ? 8 : u == 15 ? 6 : u == 16 ? 4 : 3)>();
  });
  if (MODE == 2) __builtin_amdgcn_s_setprio(2);
  drain();
  gt = *(LAS const u32x4*)(lds + 32768 + w * 4096 + r32 * 16);
#pragma unroll
  for (int i = 0; i < NT; ++i) gc[i] = *(LAS const u32x4*)(lds + 32768 + w * 4096 + (i + 1) * 512 + r32 * 16);
  for (unsigned spin = 0; spin < SPIN_CAP; ++spin) {
    unsigned bad = (gt[0] ^ tag.a) | (gt[2] ^ tag.b);
#pragma unroll
    for (int i = 0; i < NT; ++i) bad |= (gc[i][0] ^ tag.a) | (gc[i][2] ^ tag.b);
    const bool ok = bad == 0;
    if (__all(ok)) break;
    __builtin_amdgcn_s_sleep(2);
    load_stats();
  }
  if (MODE != 2 && w == 0) flag_store(rws, SY_GO + blockIdx.x * 128u, tag);
  const float mean = __uint_as_float(gt[1]);
  float rs = __uint_as_float(gt[3]);
  if (MODE == 2) rs *= __builtin_amdgcn_rsqf(__builtin_fmaf(-EPS * rs, rs, 1.0f + EPS));
  LAS float* st_a = (LAS float*)(lds + w * 4096);
  LAS float* st_b = (LAS float*)(lds + w * 4096 + 1024);
  if (MODE == 1) {
    if (hh == 0) { st_a[r32] = rs; st_b[r32] = mean * rs; }
  } else {
#pragma unroll
    for (int i = 0; i < NT; ++i) {
      if (hh == 0) st_a[i * 32 + r32] = __uint_as_float(gc[i][1]);
      else if (MODE == 2) st_b[i * 32 + r32] = bf_lo(p.b1[n0 + i * 32 + r32]);
    }
  }
  const unsigned tile = (unsigned)(panel * 4 + w);
  if constexpr (MODE == 0) {
    const bool isq = ct < 5;
    const float sc = isq ? rs * QSCALE : rs, sm = -sc * mean;
    const unsigned hstride = isq ? 64u * 1280u : 64u * 2560u;
    const unsigned base = isq ? WS_Q + tile * 1280u : WS_KV + (tile >> 1) * 5120u;
    const unsigned f0 = (isq ? 0u : (tile & 1u) * 1024u) + (unsigned)r32 * 16u;
    const unsigned f1 = isq ? 1024u + (unsigned)r32 * 8u : 2048u + (unsigned)r32 * 16u + (tile & 1u) * 8u;
    const unsigned o16 = base + (unsigned)(H0 + ((hh + od) >> 1)) * hstride + (unsigned)((hh + od) & 1) * 512u + f0;
#pragma unroll
    for (int i = 0; i < NT; ++i) {
      unsigned pk[8];
#pragma unroll
      for (int g = 0; g < 4; ++g) {
        const f32x4 c4 = *(LAS const f32x4*)(st_a + i * 32 + 8 * g + 4 * hh);
        const f32x2 lo = (f32x2){acc[i][4 * g], acc[i][4 * g + 1]} * sc + (f32x2){c4[0], c4[1]} * sm;
        const f32x2 hi = (f32x2){acc[i][4 * g + 2], acc[i][4 * g + 3]} * sc + (f32x2){c4[2], c4[3]} * sm;
        pk[2 * g] = pack2(lo[0], lo[1]); pk[2 * g + 1] = pack2(hi[0], hi[1]);
      }
      if (i < 3) {
        __builtin_amdgcn_raw_buffer_store_b128((u32x4){pk[0], pk[1], pk[2], pk[3]}, rws, o16 + (2 * i) * hstride, 0, SC1);
        __builtin_amdgcn_raw_buffer_store_b128((u32x4){pk[4], pk[5], pk[6], pk[7]}, rws, o16 + (2 * i + 1) * hstride, 0, SC1);
      } else {
#pragma unroll
        for (int g = 0; g < 4; ++g) {
          const unsigned qd = (unsigned)(32 * c5 + quad_of(3, g, hh)), head = qd / 5u, dq = qd - head * 5u;
          const unsigned off = base + head * hstride + (dq == 4u ? f1 : f0 + (dq >> 1) * 512u + (dq & 1u) * 8u);
          __builtin_amdgcn_raw_buffer_store_b64((u32x2){pk[2 * g], pk[2 * g + 1]}, rws, off, 0, SC1);
        }
      }
      SBAR();
    }
    publish(rws, SY_FQ + ((unsigned)ct * 64u + tile) * 8u, tag);
  } else if constexpr (MODE == 1) {
    f32x4 r4[4], m4[4];
#pragma unroll
    for (int g = 0; g < 4; ++g) { r4[g] = *(LAS const f32x4*)(st_a + 8 * g + 4 * hh); m4[g] = *(LAS const f32x4*)(st_b + 8 * g + 4 * hh); }
#pragma unroll
    for (int i = 0; i < NT; ++i) {
      const float c = -__uint_as_float(gc[i][1]);
      const unsigned f = (unsigned)(c5 * 128 + i * 32 + r32), head = f / 20u, d = f - head * 20u;
      float v[16];
#pragma unroll
      for (int g = 0; g < 4; ++g)
#pragma unroll
        for (int e = 0; e < 4; ++e) v[4 * g + e] = __builtin_fmaf(acc[i][4 * g + e], r4[g][e], m4[g][e] * c);
      const unsigned off = WS_KV + head * 163840u + (tile >> 1) * 5120u + 2560u + (tile & 1u) * 1280u + d * 32u + (unsigned)hh * 16u;
#pragma unroll
      for (int s = 0; s < 2; ++s)
        __builtin_amdgcn_raw_buffer_store_b128((u32x4){pack2(v[8 * s], v[8 * s + 1]), pack2(v[8 * s + 2], v[8 * s + 3]), pack2(v[8 * s + 4], v[8 * s + 5]), pack2(v[8 * s + 6], v[8 * s + 7])}, rws, off + s * 640, 0, SC1);
      SBAR();
    }
    publish(rws, SY_FQ + ((unsigned)ct * 64u + tile) * 8u, tag);
  } else {
    const float mr = -mean * rs;
    const unsigned tok = (unsigned)(panel * 128 + w * 32 + r32);
    constexpr float G0 = -2.0f * 0.7978845608028654f * 1.4426950408889634f, G1 = G0 * 0.044715f;
    const unsigned off = WS_H + ((tok >> 6) * 192u + (unsigned)(n0 / 8 + hh)) * 1024u + (tok & 63u) * 16u;
#pragma unroll
    for (int i = 0; i < NT; ++i) {
      float v[16];
      if (i == 2) { SBAR(); __builtin_amdgcn_s_setprio(0); SBAR(); }
#pragma unroll
      for (int g = 0; g < 4; ++g) {
        const f32x4 c4 = *(LAS const f32x4*)(st_a + i * 32 + 8 * g + 4 * hh), b4 = *(LAS const f32x4*)(st_b + i * 32 + 8 * g + 4 * hh);
#pragma unroll
        for (int e = 0; e < 4; e += 2) {
          const f32x2 u = (f32x2){acc[i][4 * g + e], acc[i][4 * g + e + 1]} * rs + ((f32x2){c4[e], c4[e + 1]} * mr + (f32x2){b4[e], b4[e + 1]});
          const f32x2 z = u * ((u * u) * G1 + G0);
          const f32x2 d = (f32x2){__builtin_amdgcn_exp2f(z[0]), __builtin_amdgcn_exp2f(z[1])} + 1.0f;
          const f32x2 r = u * (f32x2){__builtin_amdgcn_rcpf(d[0]), __builtin_amdgcn_rcpf(d[1])};
          v[4 * g + e] = r[0]; v[4 * g + e + 1] = r[1];
        }
      }
#pragma unroll
      for (int s = 0; s < 2; ++s)
        __builtin_amdgcn_raw_buffer_store_b128((u32x4){pack2(v[8 * s], v[8 * s + 1]), pack2(v[8 * s + 2], v[8 * s + 3]), pack2(v[8 * s + 4], v[8 * s + 5]), pack2(v[8 * s + 6], v[8 * s + 7])}, rws, off + (i * 4 + 2 * s) * 1024, 0, SC1);
    }
    publish(rws, SY_FH + (tile * 16u + (unsigned)ct) * 8u, tag);
  }
}

DI void retile_block(const Params& p, int u, lds_ptr lds, Tag tag) {
  const int tid = threadIdx.x, lane = tid & 63, w = __builtin_amdgcn_readfirstlane(tid >> 6);
  const bool is2 = u < 96;
  const int s = (is2 ? u : u - 96) >> 1, half = u & 1;
  const rsrc_t rsrc = mk_rsrc(is2 ? p.w2 : p.wo, is2 ? F_ * D_ * 2 : D_ * D_ * 2);
  const unsigned lds0 = (unsigned)(uintptr_t)lds;
  wait_bar<0>();
#pragma unroll
  for (int i = 0; i < 5; ++i) {
    const unsigned id = (unsigned)((w * 5 + i) * 64 + lane), row = id / 40u, c = id - row * 40u;
    dma16(rsrc, row * 1280u + c * 16u, (unsigned)s * 32u * 1280u + (unsigned)half * 640u, lds0 + (unsigned)(w * 5 + i) * 1024u);
  }
  wait_bar<0>();
  const int g = lane >> 4, q = (lane & 15) >> 2, pp = lane & 3;
  const unsigned ra = (unsigned)(is2 ? 16 * (g >> 1) + 4 * (g & 1) + q : 8 * g + q) * 640u + (unsigned)w * 160u + (unsigned)pp * 8u;
  const unsigned rt = is2 ? 8u * 640u : 4u * 640u;
  const unsigned cn = (unsigned)(half * 4 + w);
  const rsrc_t rws = mk_rsrc(p.ws, WS_END);
  const unsigned dst = (is2 ? WS_W2 + (cn * 48u + (unsigned)s) * 5120u : WS_WO + (cn * 20u + (unsigned)s) * 5120u) + (unsigned)lane * 16u;
#pragma unroll
  for (int j = 0; j < 5; ++j)
    __builtin_amdgcn_raw_buffer_store_b128(__builtin_bit_cast(u32x4, cat8(lds_tr(lds + ra + j * 32), lds_tr(lds + ra + rt + j * 32))), rws, dst + j * 1024, 0, SC1);
  drain();
  flag_store(rws, SY_FW + (cn * 96u + (unsigned)(is2 ? s : 48 + s)) * 8u, tag);
  wait_bar<0>();
}

DI void colsum_block(const Params& p, int e, lds_ptr lds, Tag tag) {
  const int tid = threadIdx.x, lane = tid & 63, w = tid >> 6;
  const bool isq = e < 30;
  const unsigned ldw = isq ? 3u * D_ : (unsigned)F_;
  const unsigned short* src = (isq ? p.wqkv + e * 64 : p.w1 + (e - 30) * 64) + (size_t)(tid >> 3) * 20u * ldw + (tid & 7) * 8;
  u32x4 v[20];
#pragma unroll
  for (int r = 0; r < 20; ++r) v[r] = *(const u32x4*)(src + (size_t)r * ldw);
  float s[8] = {0.f, 0.f, 0.f, 0.f, 0.f, 0.f, 0.f, 0.f};
#pragma unroll
  for (int r = 0; r < 20; ++r)
#pragma unroll
    for (int c = 0; c < 4; ++c) { s[2 * c] += bf_lo(v[r][c]); s[2 * c + 1] += bf_hi(v[r][c]); }
#pragma unroll
  for (int c = 0; c < 8; ++c) { s[c] += __shfl_xor(s[c], 8); s[c] += __shfl_xor(s[c], 16); s[c] += __shfl_xor(s[c], 32); }
  LAS float* red = (LAS float*)lds;
  if (lane < 8) {
#pragma unroll
    for (int c = 0; c < 8; ++c) red[w * 64 + lane * 8 + c] = s[c];
  }
  __syncthreads();
  if (tid < 64) {
    const unsigned t = __float_as_uint((red[tid] + red[64 + tid]) + (red[128 + tid] + red[192 + tid]));
    __builtin_amdgcn_raw_buffer_store_b128((u32x4){tag.a, t, tag.b, t}, mk_rsrc(p.ws, WS_END), SY_COL + (unsigned)(e * 64 + tid) * 16u, 0, SC1);
  }
  wait_bar<63>();
}

DI void rowstat_block(const Params& p, int b, Tag& tag) {
  const int tid = threadIdx.x, r32 = tid & 31, row = b * 8 + (tid >> 5);
  const u32x2* src = (const u32x2*)(p.x + (size_t)row * D_) + r32;
  u32x2 v[5];
#pragma unroll
  for (int j = 0; j < 5; ++j) v[j] = src[j * 32];
  tag = load_tag(p);
  const rsrc_t rws = mk_rsrc(p.ws, WS_END);
  if (tid < 64) flag_store(rws, SY_START + blockIdx.x * 8u, tag);
  float f[20], s = 0.f;
#pragma unroll
  for (int j = 0; j < 5; ++j) { f[4 * j] = bf_lo(v[j][0]); f[4 * j + 1] = bf_hi(v[j][0]); f[4 * j + 2] = bf_lo(v[j][1]); f[4 * j + 3] = bf_hi(v[j][1]); }
#pragma unroll
  for (int j = 0; j < 20; ++j) s += f[j];
#pragma unroll
  for (int m = 1; m < 32; m <<= 1) s += __shfl_xor(s, m);
  const float mean = s * (1.0f / D_);
  float q = 0.f;
#pragma unroll
  for (int j = 0; j < 20; ++j) { const float d = f[j] - mean; q = __builtin_fmaf(d, d, q); }
#pragma unroll
  for (int m = 1; m < 32; m <<= 1) q += __shfl_xor(q, m);
  const float rs = __builtin_amdgcn_rsqf(q * (1.0f / D_) + EPS);
  if (r32 == 0) __builtin_amdgcn_raw_buffer_store_b128((u32x4){tag.a, __float_as_uint(mean), tag.b, __float_as_uint(rs)}, rws, SY_TOK + (unsigned)row * 16u, 0, SC1);
}

constexpr unsigned PAIR = 5152, SLOT = 4 * PAIR;

DI void attn_store_o(rsrc_t rws, const f32x16& o, float inv, unsigned tok, int head, int hh) {
  const unsigned base = WS_O + (tok >> 6) * 80u * 1024u + (tok & 63u) * 16u;
#pragma unroll
  for (int g = 0; g < 3; ++g) {
    const unsigned quad = (unsigned)(head * 5 + 2 * g + hh);
    const unsigned off = (g == 2 && hh) ? OOB : base + (quad >> 1) * 1024u + (quad & 1) * 8u;
    __builtin_amdgcn_raw_buffer_store_b64((u32x2){pack2(o[4 * g] * inv, o[4 * g + 1] * inv), pack2(o[4 * g + 2] * inv, o[4 * g + 3] * inv)}, rws, off, 0, SC1);
  }
}
#define PF(x) __builtin_bit_cast(s16x8, x)

DI f32x16 attn_safe(rsrc_t rws, int head, s16x8 q0, s16x8 q1, int r32, int hh) {
  f32x16 o = f32x16{};
  float m = -3.0e38f;
  const unsigned kb = WS_KV + (unsigned)head * 64u * 2560u;
  const u32x4 ones = (u32x4){0x3F803F80u, 0x3F803F80u, 0x3F803F80u, 0x3F803F80u};
  for (int t = 0; t < 64; ++t) {
    const unsigned tb = kb + (unsigned)(t >> 1) * 5120u, od = (unsigned)t & 1u;
    const u32x4 k0 = __builtin_amdgcn_raw_buffer_load_b128(rws, tb + od * 1024u + (unsigned)hh * 512u + (unsigned)r32 * 16u, 0, SC1);
    const u32x2 k1 = __builtin_amdgcn_raw_buffer_load_b64(rws, tb + 2048u + (unsigned)r32 * 16u + od * 8u, 0, SC1);
    const unsigned vo = r32 < 20 ? tb + 2560u + od * 1280u + (unsigned)r32 * 32u + (unsigned)hh * 16u : OOB;
    u32x4 v0 = __builtin_amdgcn_raw_buffer_load_b128(rws, vo, 0, SC1), v1 = __builtin_amdgcn_raw_buffer_load_b128(rws, vo, 640, SC1);
    if (r32 >= 20) { v0 = ones; v1 = ones; }
    f32x16 s = MFMA32(PF(k0), q0, f32x16{});
    s = MFMA32(PF(((u32x4){k1[0], k1[1], 0u, 0u})), q1, s);
    float mx = s[0];
#pragma unroll
    for (int r = 1; r < 16; ++r) mx = fmaxf(mx, s[r]);
    mx = fmaxf(mx, __shfl_xor(mx, 32));
    const float mn = fmaxf(m, mx), al = __builtin_amdgcn_exp2f(m - mn);
    m = mn;
#pragma unroll
    for (int r = 0; r < 16; ++r) { s[r] = __builtin_amdgcn_exp2f(s[r] - mn); o[r] *= al; }
    o = MFMA32(PF(v0), PF(((u32x4){pack2(s[0], s[1]), pack2(s[2], s[3]), pack2(s[4], s[5]), pack2(s[6], s[7])})), o);
    o = MFMA32(PF(v1), PF(((u32x4){pack2(s[8], s[9]), pack2(s[10], s[11]), pack2(s[12], s[13]), pack2(s[14], s[15])})), o);
  }
  return o;
}
DI bool attn_unsafe(const f32x16& o, float l) {
  float a = 0.f;
#pragma unroll
  for (int r = 0; r < 12; ++r) a += fabsf(o[r]);
  return !(l > 9.0e-13f && l < 1.0e12f && a < 1.0e36f);
}

DI void attn_block(const Params& p, int head, int qt, lds_ptr lds, Tag tag) {
  const int tid = threadIdx.x, lane = tid & 63, w = __builtin_amdgcn_readfirstlane(tid >> 6);
  const int r32 = lane & 31, hh = lane >> 5;
  const unsigned lds0 = (unsigned)(uintptr_t)lds;
  const rsrc_t rws = mk_rsrc(p.ws, WS_END);
  const unsigned t0 = (unsigned)(5 * head) >> 5, t1 = (unsigned)(5 * head + 4) >> 5;
  if (w == 0) {
    const unsigned row = (lane & 32 ? 10u : 5u) + (lane & 16 ? t1 : t0);
    const unsigned off[2] = {SY_FQ + (row * 64u + (((unsigned)qt * 8u + ((unsigned)lane & 15u)) & 63u)) * 8u,
                             lane < 16 ? SY_FQ + ((lane < 8 ? t0 : t1) * 64u + (unsigned)(qt * 8 + (lane & 7))) * 8u : OOB};
    poll_flags<2, 6>(rws, off, tag);
  }
  wait_bar<0>();
  const unsigned kv0 = WS_KV + (unsigned)head * 64u * 2560u + (unsigned)w * 5120u;
  {
    const unsigned qb = WS_Q + ((unsigned)head * 64u + (unsigned)(qt * 8 + w * 2)) * 1280u;
#pragma unroll
    for (int i = 0; i < 3; ++i) dma16_sc1(rws, (unsigned)lane * 16u, qb + (unsigned)i * 1024u, lds0 + 3 * SLOT + (unsigned)w * 3072u + (unsigned)i * 1024u);
  }
#pragma unroll
  for (int i = 0; i < 9; ++i)
    dma16_sc1(rws, (unsigned)lane * 16u, kv0 + (unsigned)((qt + i / 5) & 7) * 20480u + (unsigned)(i % 5) * 1024u, lds0 + (unsigned)(i / 5) * SLOT + (unsigned)w * PAIR + (unsigned)(i % 5) * 1024u);
  if (tid < 24) { const unsigned c = (tid & 1) ? 0u : 0x3F803F80u; *(LAS u32x4*)(lds + (tid >> 1) * PAIR + 5120 + (tid & 1) * 16) = (u32x4){c, c, c, c}; }
  s16x8 qf0[2], qe[2], qo[2];
  const unsigned ka0 = (unsigned)hh * 512u + (unsigned)r32 * 16u, ka1 = 2048u + (unsigned)r32 * 16u;
  unsigned va[4];
#pragma unroll
  for (int i = 0; i < 4; ++i) va[i] = r32 < 20 ? 2560u + (unsigned)i * 640u + (unsigned)r32 * 32u + (unsigned)hh * 16u : (r32 == 20 || r32 == 24) ? 5120u : 5136u;
  f32x16 oa = f32x16{}, ob = f32x16{};
  f32x16 sA0, sA1, sB0, sB1;
  s16x8 k0[2], k1[2], v0[2], v1[2];
  u32x4 pa0, pa1, pb0, pb1;
#define RD_K(slot, i) do { k0[(i) & 1] = lds_b128(lds + ka0 + (slot) + ((i) >> 1) * PAIR + ((i) & 1) * 1024); \
    if (!((i) & 1)) k1[((i) >> 1) & 1] = lds_b128(lds + ka1 + (slot) + ((i) >> 1) * PAIR); } while (0)
#define RD_V(slot, i) do { v0[(i) & 1] = lds_b128(lds + va[((i) & 1) * 2] + (slot) + ((i) >> 1) * PAIR); \
    v1[(i) & 1] = lds_b128(lds + va[((i) & 1) * 2 + 1] + (slot) + ((i) >> 1) * PAIR); } while (0)
#define EX4(S, r) do { S[r] = __builtin_amdgcn_exp2f(S[r]); S[r + 1] = __builtin_amdgcn_exp2f(S[r + 1]); S[r + 2] = __builtin_amdgcn_exp2f(S[r + 2]); S[r + 3] = __builtin_amdgcn_exp2f(S[r + 3]); \
    SBAR(); } while (0)
#define CV2(P, j, S, r) do { P[j] = pack2(S[r], S[r + 1]); P[j + 1] = pack2(S[r + 2], S[r + 3]); SBAR(); } while (0)
  const f32x16 zero = f32x16{};
  wait_bar<4>();
#pragma unroll
  for (int a = 0; a < 2; ++a) {
    qf0[a] = lds_b128(lds + 3 * SLOT + w * 3072 + a * 1280 + ka0);
    const u32x2 t = *(LAS const u32x2*)(lds + 3 * SLOT + w * 3072 + a * 1280 + 1024 + r32 * 8);
    const unsigned t0 = hh ? 0u : t[0], t1 = hh ? 0u : t[1];
    qe[a] = PF(((u32x4){t0, t1, 0u, 0u})); qo[a] = PF(((u32x4){0u, 0u, t0, t1}));
  }
  RD_K(0, 0); RD_K(0, 1); RD_V(0, 0);
  __builtin_amdgcn_s_setprio(1);
  sA0 = MFMA32(k0[0], qf0[0], zero); sA0 = MFMA32(k1[0], qe[0], sA0);
  sA1 = MFMA32(k0[0], qf0[1], zero); sA1 = MFMA32(k1[0], qe[1], sA1);
  pb1 = u32x4{}; sB1 = zero; v1[1] = v0[0];
#define Q1(T, a) ((((T) + 1) & 1) ? qo[a] : qe[a])
#define STEP(C0, C1, N0, N1, T, KS, VS, D0, D1) do { \
    __builtin_amdgcn_s_waitcnt(0xC07F); SBAR(); \
    RD_K(KS, ((T) + 2) & 7); SBAR(); \
    N0 = MFMA32(k0[((T) + 1) & 1], qf0[0], zero); SBAR();                  D0; CV2(pb1, 2, N1, 12); EX4(C0, 0); \
    ob = MFMA32(v1[((T) + 1) & 1], PF(pb1), ob); SBAR();                   RD_V(VS, ((T) + 1) & 7); SBAR(); CV2(pa0, 0, C0, 0); EX4(C0, 4); \
    N0 = MFMA32(k1[(((T) + 1) >> 1) & 1], Q1(T, 0), N0); SBAR();           CV2(pa0, 2, C0, 4); EX4(C0, 8); \
    oa = MFMA32(v0[(T) & 1], PF(pa0), oa); SBAR();                         CV2(pa1, 0, C0, 8); EX4(C0, 12); \
    N1 = MFMA32(k0[((T) + 1) & 1], qf0[1], zero); SBAR();                  D1; CV2(pa1, 2, C0, 12); EX4(C1, 0); \
    oa = MFMA32(v1[(T) & 1], PF(pa1), oa); SBAR();                         CV2(pb0, 0, C1, 0); EX4(C1, 4); \
    N1 = MFMA32(k1[(((T) + 1) >> 1) & 1], Q1(T, 1), N1); SBAR();           CV2(pb0, 2, C1, 4); EX4(C1, 8); \
    ob = MFMA32(v0[(T) & 1], PF(pb0), ob); SBAR();                         CV2(pb1, 0, C1, 8); EX4(C1, 12); \
  } while (0)
#define KVP(cc, sl, i) do { dma16_sc1(mk_rsrc(p.ws, (cc) < 8 ? WS_END : 0), (unsigned)lane * 16u, kv0 + (unsigned)((qt + (cc)) & 7) * 20480u + (i) * 1024u, lds0 + (sl) + (unsigned)w * PAIR + (i) * 1024u); SBAR(); } while (0)
#define NOP_ do { } while (0)
#define HINT do { __builtin_amdgcn_raw_buffer_store_b64((u32x2){tag.a, tag.b}, rws, (c == MLP2_GATE && tid == 0) ? SY_GO + 64u + blockIdx.x * 128u : OOB, 0, SC1); SBAR(); } while (0)
#define FLG do { dma16_half_sc1(mk_rsrc(p.ws, c == 0 ? WS_END : 0), (unsigned)lane * 16u, SY_FQ + ((w & 2 ? 10u : 5u) + (w & 1 ? t1 : t0)) * 512u, lds0 + 3 * SLOT + 12288u + (unsigned)w * 512u); SBAR(); } while (0)
  unsigned cur = 0, nxt = SLOT, prv = 2 * SLOT;
  for (int c = 0; c < 8; ++c) {
    STEP(sA0, sA1, sB0, sB1, 0, cur, cur, KVP(c + 1, nxt, 4), NOP_);
    STEP(sB0, sB1, sA0, sA1, 1, cur, cur, HINT, NOP_);
    STEP(sA0, sA1, sB0, sB1, 2, cur, cur, NOP_, NOP_);
    STEP(sB0, sB1, sA0, sA1, 3, cur, cur, NOP_, NOP_);
    STEP(sA0, sA1, sB0, sB1, 4, cur, cur, FLG, NOP_);
    STEP(sB0, sB1, sA0, sA1, 5, cur, cur, NOP_, NOP_);
    wait_bar<0>();
    if (c == 0) {
      const u32x4 fa = *(LAS const u32x4*)(lds + 3 * SLOT + 12288 + lane * 16), fb = *(LAS const u32x4*)(lds + 3 * SLOT + 13312 + lane * 16);
      const unsigned bad = (fa[0] ^ tag.a) | (fa[1] ^ tag.b) | (fa[2] ^ tag.a) | (fa[3] ^ tag.b) | (fb[0] ^ tag.a) | (fb[1] ^ tag.b) | (fb[2] ^ tag.a) | (fb[3] ^ tag.b);
      if (__builtin_expect(__any(bad != 0u), 0)) {
        const unsigned l8 = (unsigned)lane * 8u;
        const unsigned off[4] = {SY_FQ + (5 + t0) * 512u + l8, SY_FQ + (5 + t1) * 512u + l8, SY_FQ + (10 + t0) * 512u + l8, SY_FQ + (10 + t1) * 512u + l8};
        poll_flags<4>(rws, off, tag);
      }
    }
    STEP(sA0, sA1, sB0, sB1, 6, nxt, cur, KVP(c + 2, prv, 0), KVP(c + 2, prv, 1));
    STEP(sB0, sB1, sA0, sA1, 7, nxt, nxt, KVP(c + 2, prv, 2), KVP(c + 2, prv, 3));
    const unsigned t_ = cur; cur = nxt; nxt = prv; prv = t_;
  }
  __builtin_amdgcn_s_setprio(3);
  CV2(pb1, 2, sB1, 12);
  ob = MFMA32(v1[1], PF(pb1), ob);
  float la = hh ? oa[8] : oa[12], lb = hh ? ob[8] : ob[12];
  if (__builtin_expect(__any(attn_unsafe(oa, la) || attn_unsafe(ob, lb)), 0)) {
    oa = attn_safe(rws, head, qf0[0], qe[0], r32, hh);
    ob = attn_safe(rws, head, qf0[1], qe[1], r32, hh);
    la = hh ? oa[8] : oa[12]; lb = hh ? ob[8] : ob[12];
  }
  const unsigned tok = (unsigned)(qt * 256 + w * 64 + r32);
  attn_store_o(rws, oa, __builtin_amdgcn_rcpf(la), tok, head, hh);
  attn_store_o(rws, ob, __builtin_amdgcn_rcpf(lb), tok + 32, head, hh);
  publish(rws, SY_FO + ((unsigned)(qt * 4 + w) * 32u + (unsigned)head) * 8u, tag);
}

constexpr unsigned ST2 = 18432;
DI void tail_block(const Params& p, int rt, int cn, lds_ptr lds, Tag tag) {
  const int tid = threadIdx.x, lane = tid & 63, w = __builtin_amdgcn_readfirstlane(tid >> 6);
  const unsigned lds0 = (unsigned)(uintptr_t)lds;
  const rsrc_t rws = mk_rsrc(p.ws, WS_END);
  const unsigned n0 = (unsigned)cn * 80u, l16 = (unsigned)lane * 16u;
  const int g = lane >> 4;
  const unsigned afrag = (unsigned)g * 1024u + (unsigned)(w * 16 + (lane & 15)) * 16u;
  const unsigned tok = (unsigned)(rt * 64 + w * 16 + (lane & 15));
  u32x2 xb[5], bb[5];
#pragma unroll
  for (int j = 0; j < 5; ++j) { xb[j] = *(const u32x2*)(p.x + tok * D_ + n0 + j * 16 + g * 4); bb[j] = *(const u32x2*)(p.b2 + n0 + j * 16 + g * 4); }
  if (w == 0) {
    const unsigned off[2] = {lane < 32 ? SY_FH + ((unsigned)rt * 32u + (unsigned)lane) * 8u : lane < 36 ? SY_FW + ((unsigned)cn * 96u + 32u + (unsigned)lane) * 8u : OOB,
                             SY_FW + ((unsigned)cn * 96u + (unsigned)lane) * 8u};
    poll_flags<2, 6>(rws, off, tag);
  }
  wait_bar<0>();
  __builtin_amdgcn_s_setprio(0);
  const unsigned wo0 = WS_WO + (unsigned)cn * 102400u;
  u32x4 wr[4][5];
#pragma unroll
  for (int s = 0; s < 4; ++s)
#pragma unroll
    for (int j = 0; j < 5; ++j) wr[s][j] = __builtin_amdgcn_raw_buffer_load_b128(rws, l16, wo0 + (unsigned)((16 + s) * 5 + j) * 1024u, SC1);
#pragma unroll
  for (int s = 0; s < 4; ++s)
#pragma unroll
    for (int j = 0; j < 5; ++j) asm volatile("" : "+v"(wr[s][j]));
#pragma unroll
  for (int j = 0; j < 5; ++j) asm volatile("" : "+v"(xb[j]), "+v"(bb[j]));
  f32x4 am[5], as[5];
#pragma unroll
  for (int j = 0; j < 5; ++j) { am[j] = f32x4{}; as[j] = f32x4{}; }
  {
    const unsigned a0 = WS_H + (unsigned)rt * 192u * 1024u, w0 = WS_W2 + (unsigned)cn * 245760u;
    auto issue1 = [&](int st, unsigned slot, bool wo, int i) {
      if (i == 4 && (w >> 1) != (st & 1)) return;
      const unsigned d = i < 2 ? (unsigned)w * 2048u + (unsigned)i * 1024u : i < 4 ? 8192u + (unsigned)(i - 2) * 4096u + (unsigned)w * 1024u : 16384u + (unsigned)(w & 1) * 1024u;
      const unsigned src = wo ? wo0 + slot + d : i < 2 ? a0 + (unsigned)st * 8192u + (unsigned)i * 4096u : w0 + (unsigned)st * 10240u + d - 8192u;
      dma16_sc1(rws, (i < 2 && !wo) ? afrag : l16, src, lds0 + slot + d);
    };
    auto issue = [&](int st, unsigned slot, bool wo) {
#pragma unroll
      for (int i = 0; i < 5; ++i) issue1(st, slot, wo, i);
    };
    dma16_sc1(rws, l16, wo0 + 4 * ST2 + (unsigned)w * 2048u, lds0 + 4 * ST2 + (unsigned)w * 2048u);
    dma16_sc1(rws, l16, wo0 + 4 * ST2 + (unsigned)w * 2048u + 1024u, lds0 + 4 * ST2 + (unsigned)w * 2048u + 1024u);
    issue(0, 0, false); issue(1, ST2, false); issue(2, 2 * ST2, false); issue(3, 3 * ST2, false);
    const unsigned aa = (unsigned)w * 2048u + l16;
    s16x8 af[2][2], wf[2][10];
    if (w == 0) {
      const unsigned off[1] = {lane == 0 ? SY_GO + 64u + (blockIdx.x & 255u) * 128u : OOB};
      poll_flags<1, 16>(rws, off, tag);
    }
    wait_bar<13>();
#pragma unroll
    for (int s = 0; s < 2; ++s) af[0][s] = lds_b128(lds + aa + s * 1024);
#pragma unroll
    for (int f = 0; f < 10; ++f) wf[0][f] = lds_b128(lds + l16 + 8192 + f * 1024);
    wait_bar<9>();
    auto stage = [&](int st, auto Uc, bool wo) {
      constexpr int U = decltype(Uc)::value;
      constexpr unsigned nx = ((U + 1) & 3) * ST2;
#pragma unroll
      for (int m = 0; m < 10; ++m) {
        am[m % 5] = MFMA16(wf[U & 1][m], af[U & 1][m / 5], am[m % 5]);
        SBAR();
        if (m == 0) { af[(U + 1) & 1][0] = lds_b128(lds + aa + nx); af[(U + 1) & 1][1] = lds_b128(lds + aa + nx + 1024); }
        if (m >= 1 && m < 6) { wf[(U + 1) & 1][2 * m - 2] = lds_b128(lds + l16 + nx + 8192 + (2 * m - 2) * 1024); wf[(U + 1) & 1][2 * m - 1] = lds_b128(lds + l16 + nx + 8192 + (2 * m - 1) * 1024); }
        SBAR();
        if (m & 1) issue1(st + 4, U * ST2, wo, m >> 1);
        SBAR();
      }
      wait_bar<9>();
    };
    for (int st = 0; st < 20; st += 4) {
      stage(st, std::integral_constant<int, 0>{}, false); stage(st + 1, std::integral_constant<int, 1>{}, false);
      stage(st + 2, std::integral_constant<int, 2>{}, false); stage(st + 3, std::integral_constant<int, 3>{}, false);
    }
    stage(20, std::integral_constant<int, 0>{}, true); stage(21, std::integral_constant<int, 1>{}, true);
    stage(22, std::integral_constant<int, 2>{}, true); stage(23, std::integral_constant<int, 3>{}, true);
  }
  __builtin_amdgcn_s_setprio(2);
#pragma unroll
  for (int j = 0; j < 5; ++j) {
#pragma unroll
    for (int e = 0; e < 4; ++e) {
      const float m = am[j][e] + (e & 1 ? bf_hi(bb[j][e >> 1]) : bf_lo(bb[j][e >> 1]));
      am[j][e] = sigmoid_mul(m, m * -1.4426950408889634f);
    }
    asm volatile("" : "+v"(am[j]));
  }
  wait_bar<0>();
  {
    const unsigned fo = lane < 32 ? SY_FO + ((unsigned)rt * 32u + (unsigned)lane) * 8u : OOB;
    f32x4 a2[5];
#pragma unroll
    for (int j = 0; j < 5; ++j) a2[j] = f32x4{};
    u32x4 of[20];
    auto request = [&](auto Gc) {
      constexpr int G = decltype(Gc)::value;
#pragma unroll
      for (int s = 5 * G; s < 5 * G + 5; ++s) of[s] = __builtin_amdgcn_raw_buffer_load_b128(rws, afrag, WS_O + (unsigned)rt * 81920u + (unsigned)s * 4096u, SC1);
    };
    auto compute = [&](auto Gc) {
      constexpr int G = decltype(Gc)::value;
#pragma unroll
      for (int s = 5 * G; s < 5 * G + 5; ++s)
#pragma unroll
        for (int j = 0; j < 5; ++j) {
          const s16x8 wf = s < 16 ? lds_b128(lds + l16 + (s * 5 + j) * 1024) : PF(wr[s < 16 ? 0 : s - 16][j]);
          if (G < 2) as[j] = MFMA16(wf, PF(of[s]), as[j]); else a2[j] = MFMA16(wf, PF(of[s]), a2[j]);
        }
    };
    auto missing_now = [&]() -> unsigned {
      const u32x2 v = __builtin_amdgcn_raw_buffer_load_b64(rws, fo, 0, SC1);
      const unsigned m = (unsigned)__ballot(lane < 32 && (((v[0] ^ tag.a) | (v[1] ^ tag.b)) != 0u));
      asm volatile("" ::: "memory");
      return m;
    };
    using I0 = std::integral_constant<int, 0>; using I1 = std::integral_constant<int, 1>;
    using I2 = std::integral_constant<int, 2>; using I3 = std::integral_constant<int, 3>;
    unsigned missing = missing_now();
    if (missing == 0u) {
      request(I0{}); request(I2{}); request(I1{}); request(I3{});
      SBAR();
      compute(I0{}); compute(I2{}); compute(I1{}); compute(I3{});
    } else {
#pragma unroll
      for (int s = 0; s < 20; ++s) of[s] = u32x4{};
      unsigned req = 0u, done = 0u;
      for (unsigned spin = 0;; ++spin) {
        const unsigned r0 = req;
        if (!(req & 1u) && !(missing & 0x000000FFu)) { request(I0{}); req |= 1u; }
        if (!(req & 2u) && !(missing & 0x0000FF00u)) { request(I1{}); req |= 2u; }
        if (!(req & 4u) && !(missing & 0x00FF0000u)) { request(I2{}); req |= 4u; }
        if (!(req & 8u) && !(missing & 0xFF000000u)) { request(I3{}); req |= 8u; }
        if ((req & ~done & 1u)) { compute(I0{}); done |= 1u; }
        if ((req & ~done & 4u)) { compute(I2{}); done |= 4u; }
        if ((req & ~done & 2u) && (done & 1u)) { compute(I1{}); done |= 2u; }
        if ((req & ~done & 8u) && (done & 4u)) { compute(I3{}); done |= 8u; }
        if (done == 15u) break;
        if (req == r0) { if (__builtin_popcount(missing) > 6) __builtin_amdgcn_s_sleep(24); else __builtin_amdgcn_s_sleep(3); }
        missing = spin < SPIN_CAP ? missing_now() : 0u;
      }
    }
#pragma unroll
    for (int j = 0; j < 5; ++j) as[j] += a2[j];
  }
#pragma unroll
  for (int j = 0; j < 5; ++j) {
    float r[4];
#pragma unroll
    for (int e = 0; e < 4; ++e) r[e] = __builtin_fmaf(as[j][e], am[j][e], e & 1 ? bf_hi(xb[j][e >> 1]) : bf_lo(xb[j][e >> 1]));
    __builtin_amdgcn_raw_buffer_store_b64((u32x2){pack2(r[0], r[1]), pack2(r[2], r[3])}, mk_rsrc(p.out, S_ * D_ * 2), (tok * D_ + n0 + j * 16 + g * 4) * 2u, 0, 0);
  }
}

__global__ __launch_bounds__(256, 2) void fused_block(Params p) {
  extern __shared__ __attribute__((aligned(16))) char smem[];
  lds_ptr lds = (lds_ptr)smem;
  const int b = blockIdx.x & 255, xcd = b & 7, j = b >> 3;
  const int xq = (xcd + 5) & 7;
  const int panel = (xq & 1) * 8 + (j & 7), c = (xq >> 1) * 4 + (j >> 3);
  Tag tag;
  if (blockIdx.x < 256) {
    __builtin_amdgcn_s_setprio(3);
    if (c < 10) gemm_xw_tile<0>(p, panel, c, lds, tag);
    else if (c < 15) gemm_xw_tile<1>(p, panel, c, lds, tag);
    else {
      const rsrc_t rws = mk_rsrc(p.ws, WS_END);
      tag = load_tag(p);
      if (threadIdx.x < 64) flag_store(rws, SY_START + blockIdx.x * 8u, tag);
      colsum_block(p, panel, lds, tag);
      colsum_block(p, 30 + panel, lds, tag);
      if (panel == 15 && threadIdx.x < 64) {
        unsigned off[8];
#pragma unroll
        for (int i = 0; i < 8; ++i) off[i] = SY_START + (unsigned)(i * 64 + (int)threadIdx.x) * 8u;
        poll_flags<8, 16>(rws, off, tag);
        flag_store(rws, SY_GEN, tag);
      }
    }
    attn_block(p, xcd * 4 + (j >> 3), j & 7, lds, tag);
    asm volatile(".Lpf_end:");
  } else {
    __builtin_amdgcn_s_setprio(2);
    unsigned cpf = 0;
    {
      unsigned long long pc; int len;
      asm volatile("s_getpc_b64 %0\n.Lpf_here:\n\ts_mov_b32 %1, .Lpf_end-.Lpf_here" : "=s"(pc), "=s"(len));
      cpf = __builtin_amdgcn_raw_buffer_load_b32(mk_rsrc((const void*)pc, len > 0 ? (unsigned)len : 0u), threadIdx.x < 64 ? (threadIdx.x * 32u + (unsigned)j) * 128u : OOB, 0, 0);
    }
    rowstat_block(p, b, tag);
    if (c == 15 && panel < 14) colsum_block(p, 16 + panel, lds, tag);
    if (c == 15 && panel >= 8) colsum_block(p, 38 + panel, lds, tag);
    if (b < 136) retile_block(p, b, lds, tag);
    asm volatile("" ::"v"(cpf));
    __builtin_amdgcn_s_setprio(2);
    gemm_xw_tile<2>(p, panel, c, lds, tag);
    tail_block(p, (xcd >> 1) * 8 + (j & 7), (xcd & 1) * 4 + (j >> 3), lds, tag);
  }
}

extern "C" void kernel_launch(void* const* d_in, const int* in_sizes, int n_in, void* d_out, int out_size, void* d_ws, size_t ws_size, hipStream_t stream) {
  Params p;
  p.x = (const unsigned short*)d_in[0];
  p.wqkv = (const unsigned short*)d_in[1];
  p.wo = (const unsigned short*)d_in[2];
  p.w1 = (const unsigned short*)d_in[4];
  p.b1 = (const unsigned short*)d_in[5];
  p.w2 = (const unsigned short*)d_in[6];
  p.b2 = (const unsigned short*)d_in[7];
  p.out = (unsigned short*)d_out;
  p.ws = (char*)d_ws;
  (void)hipFuncSetAttribute((const void*)fused_block, hipFuncAttributeMaxDynamicSharedMemorySize, LDS_BYTES);
  fused_block<<<512, 256, LDS_BYTES, stream>>>(p);
}
```
